```python
import torch

def _softmax(x, axis=-1):
    return torch.softmax(x, dim=axis)
def _gelu(x):
    return torch.nn.functional.gelu(x, approximate="tanh")
def _silu(x):
    return torch.nn.functional.silu(x)
def _sigmoid(x):
    return torch.sigmoid(x)
def _layernorm(x, axis=-1, eps=1e-5):
    m = x.mean(dim=axis, keepdim=True)
    v = x.var(dim=axis, keepdim=True, unbiased=False)
    return (x - m) / torch.sqrt(v + eps)
def _scatter_add(tgt, idx, src, axis=0):
    return tgt.clone().index_add_(axis, idx, src)
def _segment_sum(src, seg, n):
    out = torch.zeros((n,) + src.shape[1:], dtype=src.dtype)
    return out.index_add_(0, seg, src)
def _pad(x, pw):
    flat = tuple(v for pair in reversed(pw) for v in pair)
    return torch.nn.functional.pad(x, flat)

kernel_name = 'concat_heads__gqa_block_cached__mha_block_cached__near_ridge'

def setup_inputs():

    _w = (lambda a: (lambda t: t.to(torch.bfloat16) if t.is_floating_point() else t)(torch.as_tensor(a)))
    def _A_setup():
        B, Sq, D = 16, 48, 512
        Hq, Hkv, Sk, Dh = 32, 8, 1024, 128
        assert Hq % Hkv == 0, (Hq, Hkv)
        return {
            "x": _w((lambda *s: torch.randn(*s, dtype=torch.bfloat16) * 0.8)(B, Sq, D)),
            "k_cache": _w((lambda *s: torch.randn(*s, dtype=torch.bfloat16) * 0.8)(B, Hkv, Sk, Dh)),
            "v_cache": _w((lambda *s: torch.randn(*s, dtype=torch.bfloat16) * 0.05)(B, Hkv, Sk, Dh)),
            "wq": _w((lambda *s: torch.randn(*s, dtype=torch.bfloat16) * 0.8)(D, Hq * Dh) * (D ** -0.5)),
            "wo": _w((lambda *s: torch.randn(*s, dtype=torch.bfloat16) * 0.05)(Hq * Dh, D) * ((Hq * Dh) ** -0.5)),
        }
    _A_in = _A_setup()

    def _B_setup():
        B, Sq, D = 16, 48, 512
        H, Sk, Dh = 32, 1024, 128
        return {
            "x": _w((lambda *s: torch.randn(*s, dtype=torch.bfloat16) * 0.8)(B, Sq, D)),
            "k_cache": _w((lambda *s: torch.randn(*s, dtype=torch.bfloat16) * 0.8)(B, H, Sk, Dh)),
            "v_cache": _w((lambda *s: torch.randn(*s, dtype=torch.bfloat16) * 0.05)(B, H, Sk, Dh)),
            "wq": _w((lambda *s: torch.randn(*s, dtype=torch.bfloat16) * 0.8)(D, H * Dh) * (D ** -0.5)),
            "wo": _w((lambda *s: torch.randn(*s, dtype=torch.bfloat16) * 0.05)(H * Dh, D) * ((H * Dh) ** -0.5)),
        }
    _B_in = _B_setup()

    return {
        "A_x": _A_in["x"],
        "A_k_cache": _A_in["k_cache"],
        "A_v_cache": _A_in["v_cache"],
        "A_wq": _A_in["wq"],
        "A_wo": _A_in["wo"],
        "B_x": _B_in["x"],
        "B_k_cache": _B_in["k_cache"],
        "B_v_cache": _B_in["v_cache"],
        "B_wq": _B_in["wq"],
        "B_wo": _B_in["wo"],
    }

def reference(A_x, A_k_cache, A_v_cache, A_wq, A_wo, B_x, B_k_cache, B_v_cache, B_wq, B_wo):
    def _A_ref(x, k_cache, v_cache, wq, wo):
        B, Sq, D = x.shape
        Hq, Dh = 32, 128
        Hkv = k_cache.shape[1]
        rep = Hq // Hkv
        q = (lambda x, *d: x.permute(*d))((x @ wq).reshape(B, Sq, Hq, Dh), 0, 2, 1, 3)
        k = (lambda x, n, axis: torch.repeat_interleave(x, n, dim=axis))(k_cache, rep, axis=1)
        v = (lambda x, n, axis: torch.repeat_interleave(x, n, dim=axis))(v_cache, rep, axis=1)
        scores = torch.einsum("bhqd,bhkd->bhqk", q, k) * (Dh ** -0.5)
        p = _softmax(scores, axis=-1)
        o = torch.einsum("bhqk,bhkd->bhqd", p, v)
        return (lambda x, *d: x.permute(*d))(o, 0, 2, 1, 3).reshape(B, Sq, Hq * Dh) @ wo

    def _B_ref(x, k_cache, v_cache, wq, wo):
        B, Sq, D = x.shape
        H, Dh = 32, 128
        q = (lambda x, *d: x.permute(*d))((x @ wq).reshape(B, Sq, H, Dh), 0, 2, 1, 3)
        scores = torch.einsum("bhqd,bhkd->bhqk", q, k_cache) * (Dh ** -0.5)
        p = _softmax(scores, axis=-1)
        o = torch.einsum("bhqk,bhkd->bhqd", p, v_cache)
        return (lambda x, *d: x.permute(*d))(o, 0, 2, 1, 3).reshape(B, Sq, H * Dh) @ wo

    a = _A_ref(A_x, A_k_cache, A_v_cache, A_wq, A_wo)
    b = _B_ref(B_x, B_k_cache, B_v_cache, B_wq, B_wo)
    return torch.concatenate([a, b], axis=-1)
```

```cpp
#include <hip/hip_runtime.h>
#include <hip/hip_bf16.h>
#include <stdint.h>

typedef __attribute__((ext_vector_type(8))) __bf16 bf16x8;
typedef __attribute__((ext_vector_type(2))) __bf16 bf16x2;
typedef __attribute__((ext_vector_type(4))) short s16x4;
typedef __attribute__((ext_vector_type(4))) float f32x4;
typedef __attribute__((ext_vector_type(2))) float f32x2;
typedef __attribute__((ext_vector_type(4))) unsigned u32x4;
typedef __attribute__((ext_vector_type(2))) unsigned u32x2;
#define LAS __attribute__((address_space(3)))

#define KV_NT 1
#define NT_STR " nt"

constexpr int SQ = 48, DM = 512, NH = 32, SK = 1024, DH = 128, HD = NH * DH;
constexpr int NBLK = SK / 32;
constexpr int BLK_BYTES = 16384;
constexpr int RB = 3, RA = 6;
constexpr int LDS_ATTN = 9 * BLK_BYTES;
constexpr float SCL = 0.088388347648318447f * 1.4426950408889634f;
constexpr float RESCALE_THR = 6.0f;

__device__ __forceinline__ unsigned pack2(float lo, float hi) {
  f32x2 f = {lo, hi};
  return __builtin_bit_cast(unsigned, __builtin_convertvector(f, bf16x2));
}
__device__ __forceinline__ f32x4 mfma16(u32x4 a, u32x4 b, f32x4 c) {
  return __builtin_amdgcn_mfma_f32_16x16x32_bf16(__builtin_bit_cast(bf16x8, a), __builtin_bit_cast(bf16x8, b), c, 0, 0, 0);
}
__device__ __forceinline__ float xg_max(float v) {
  auto r = __builtin_amdgcn_permlane16_swap(__float_as_uint(v), __float_as_uint(v), false, false);
  v = fmaxf(__uint_as_float(r[0]), __uint_as_float(r[1]));
  auto s = __builtin_amdgcn_permlane32_swap(__float_as_uint(v), __float_as_uint(v), false, false);
  return fmaxf(__uint_as_float(s[0]), __uint_as_float(s[1]));
}
__device__ __forceinline__ float xg_sum(float v) {
  auto r = __builtin_amdgcn_permlane16_swap(__float_as_uint(v), __float_as_uint(v), false, false);
  v = __uint_as_float(r[0]) + __uint_as_float(r[1]);
  auto s = __builtin_amdgcn_permlane32_swap(__float_as_uint(v), __float_as_uint(v), false, false);
  return __uint_as_float(s[0]) + __uint_as_float(s[1]);
}

__device__ __forceinline__ void dma4(const char* g, unsigned m, unsigned o0, unsigned o1, unsigned o2, unsigned o3) {
  unsigned keep;
  asm volatile(
      "s_nop 4\n\ts_mov_b32 %0, m0\n\ts_mov_b32 m0, %1\n\ts_nop 0\n\t"
      "global_load_lds_dwordx4 %3, %2" NT_STR "\n\t"
      "global_load_lds_dwordx4 %4, %2 offset:1024" NT_STR "\n\t"
      "global_load_lds_dwordx4 %5, %2 offset:2048" NT_STR "\n\t"
      "global_load_lds_dwordx4 %6, %2 offset:3072" NT_STR "\n\t"
      "s_mov_b32 m0, %0"
      : "=&s"(keep)
      : "s"(m), "s"(g), "v"(o0), "v"(o1), "v"(o2), "v"(o3)
      : "memory");
}
__device__ __forceinline__ void dma2(const char* g, unsigned m, unsigned o0, unsigned o1) {
  unsigned keep;
  asm volatile(
      "s_nop 4\n\ts_mov_b32 %0, m0\n\ts_mov_b32 m0, %1\n\ts_nop 0\n\t"
      "global_load_lds_dwordx4 %3, %2" NT_STR "\n\t"
      "global_load_lds_dwordx4 %4, %2 offset:1024" NT_STR "\n\t"
      "s_mov_b32 m0, %0"
      : "=&s"(keep)
      : "s"(m), "s"(g), "v"(o0), "v"(o1)
      : "memory");
}
#define WAIT_VM(n) asm volatile("s_waitcnt vmcnt(" #n ")" ::: "memory")
#define WAIT_LGKM0() asm volatile("s_waitcnt lgkmcnt(0)" ::: "memory")
#define BAR() asm volatile("s_barrier" ::: "memory")

typedef __amdgpu_buffer_rsrc_t rsrc_t;
__device__ __forceinline__ rsrc_t mkrs(const void* p, int bytes) { return __builtin_amdgcn_make_buffer_rsrc((void*)p, (short)0, bytes, 0x00020000); }
__device__ __forceinline__ u32x4 bld128(rsrc_t rs, unsigned vo, unsigned so) { return __builtin_amdgcn_raw_buffer_load_b128(rs, vo, so, 0); }
__device__ __forceinline__ u32x2 bld64(rsrc_t rs, unsigned vo, unsigned so) { return __builtin_amdgcn_raw_buffer_load_b64(rs, vo, so, 0); }

struct LaneOffs { unsigned k0, k1, k2, k3, v0, v1; };
__device__ __forceinline__ LaneOffs lane_offs(int lane) {
  const unsigned g = lane >> 4, r = lane & 15;
  LaneOffs o;
  o.k0 = g * 256 + 16 * (r ^ ((g << 2) | 0));
  o.k1 = g * 256 + 16 * (r ^ ((g << 2) | 1));
  o.k2 = g * 256 + 16 * (r ^ ((g << 2) | 2));
  o.k3 = g * 256 + 16 * (r ^ ((g << 2) | 3));
  o.v0 = g * 256 + 16 * (r ^ (g << 1));
  o.v1 = g * 256 + 16 * (r ^ ((4 + g) << 1));
  return o;
}
__device__ __forceinline__ void dma_k_full(const char* kg, unsigned m, const LaneOffs& o) {
  dma4(kg, m, o.k0, o.k1, o.k2, o.k3);
  dma4(kg + 4096, m + 4096, o.k0, o.k1, o.k2, o.k3);
}
__device__ __forceinline__ void dma_v_full(const char* vg, unsigned m, const LaneOffs& o) {
  dma4(vg, m, o.v0, o.v1, o.v0, o.v1);
  dma4(vg + 4096, m + 4096, o.v0, o.v1, o.v0, o.v1);
}

struct AttnArgs {
  const char* x[2];
  const char* k[2];
  const char* v[2];
  const char* wq[2];
  char* o;
};

__global__ __launch_bounds__(512, 2) void attn_kernel(AttnArgs a) {
  extern __shared__ __attribute__((aligned(16))) char smem[];
  const int lane = threadIdx.x & 63;
  const int w = __builtin_amdgcn_readfirstlane(threadIdx.x >> 6);
  const int xcd = blockIdx.x & 7, j = blockIdx.x >> 3;
  const bool type1 = j < 16;
  const int b = j & 15;
  const unsigned lds0 = (unsigned)(uintptr_t)(LAS char*)smem;

  if (w >= 5) {
    const LaneOffs lo = lane_offs(lane);
    unsigned t0 = 0, t1 = 0, t2 = 0, t3 = 0, t4 = 0, t5 = 0, t6 = 0, t7 = 0, t8 = 0, t9 = 0;
    if (w == 7) {
      const unsigned woff = (unsigned)((16 * j + (lane >> 3)) * (HD * 2) + xcd * 1024 + (lane & 7) * 128);
      const char* xb = a.x[type1 ? 0 : 1] + (size_t)b * (SQ * DM * 2);
      const unsigned xoff = lane * 128;
      asm volatile(
          "s_nop 4\n\t"
          "global_load_dword %0, %10, %12\n\tglobal_load_dword %1, %11, %12\n\t"
          "global_load_dword %2, %10, %13\n\tglobal_load_dword %3, %11, %13\n\t"
          "global_load_dword %4, %14, %20\n\tglobal_load_dword %5, %15, %20\n\tglobal_load_dword %6, %16, %20\n\t"
          "global_load_dword %7, %17, %20\n\tglobal_load_dword %8, %18, %20\n\tglobal_load_dword %9, %19, %20"
          : "=&v"(t0), "=&v"(t1), "=&v"(t2), "=&v"(t3), "=&v"(t4), "=&v"(t5), "=&v"(t6), "=&v"(t7), "=&v"(t8), "=&v"(t9)
          : "v"(woff), "v"(woff + 8u * HD * 2), "s"(a.wq[0]), "s"(a.wq[1]),
            "v"(xoff), "v"(xoff + 8192u), "v"(xoff + 16384u), "v"(xoff + 24576u), "v"(xoff + 32768u), "v"(xoff + 40960u), "s"(xb)
          : "memory");
    }
    const bool ringA = type1 && w != 7;
    size_t sbase;
    unsigned ring;
    int blk0, bstep;
    if (ringA) { sbase = (size_t)(b * 8 + xcd); ring = lds0; blk0 = w - 5; bstep = 2; }
    else if (type1) { sbase = (size_t)(b * 32 + 4 * xcd); ring = lds0 + RA * BLK_BYTES; blk0 = 0; bstep = 1; }
    else { sbase = (size_t)(b * 32 + 4 * xcd + 1 + (w - 5)); ring = lds0 + (w - 5) * RB * BLK_BYTES; blk0 = 0; bstep = 1; }
    const char* kg = a.k[ringA ? 0 : 1] + sbase * (SK * DH * 2);
    const char* vg = a.v[ringA ? 0 : 1] + sbase * (SK * DH * 2);
#pragma unroll
    for (int t = 0; t < 3; ++t) {
      const int blk = blk0 + t * bstep;
      dma_k_full(kg + blk * 8192, ring + blk * BLK_BYTES, lo);
      dma_v_full(vg + blk * 8192, ring + blk * BLK_BYTES + 8192, lo);
    }
    if (!ringA) {
      WAIT_VM(32); BAR(); WAIT_VM(16); BAR(); WAIT_VM(0); BAR();
    } else if (w == 5) {
      WAIT_VM(32); BAR(); BAR(); WAIT_VM(16); BAR(); BAR(); WAIT_VM(0); BAR();
    } else {
      BAR(); WAIT_VM(32); BAR(); BAR(); WAIT_VM(16); BAR(); BAR(); WAIT_VM(0); BAR();
    }
    asm volatile("" ::"v"(t0), "v"(t1), "v"(t2), "v"(t3), "v"(t4), "v"(t5), "v"(t6), "v"(t7), "v"(t8), "v"(t9));
    return;
  }
  if (!type1 && w >= 3) return;

  const bool roleA = type1 && w < 4;
  const int p = roleA ? 0 : 1;
  const int h = roleA ? 4 * xcd + w : (type1 ? 4 * xcd : 4 * xcd + 1 + w);
  const size_t sidx = roleA ? (size_t)(b * 8 + xcd) : (size_t)(b * 32 + h);
  const char* kg = a.k[p] + sidx * (SK * DH * 2);
  const char* vg = a.v[p] + sidx * (SK * DH * 2);
  const unsigned ringoff = roleA ? 0u : (type1 ? (unsigned)(RA * BLK_BYTES) : (unsigned)(w * RB * BLK_BYTES));
  const int rdepth = roleA ? RA : RB;

  u32x4 qf[3][4];
  {
    f32x4 acc[3][8];
#pragma unroll
    for (int t = 0; t < 3; ++t)
#pragma unroll
      for (int e = 0; e < 8; ++e) acc[t][e] = f32x4{0.f, 0.f, 0.f, 0.f};
    const rsrc_t rw = mkrs(a.wq[p] + h * 256, DM * HD * 2 - h * 256);
    const rsrc_t rx = mkrs(a.x[p] + b * (SQ * DM * 2), SQ * DM * 2);
    const unsigned wv = (unsigned)((lane >> 4) * (8 * HD * 2) + (lane & 15) * 16);
    const unsigned xv = (unsigned)((lane & 15) * (DM * 2) + (lane >> 4) * 16);
    u32x4 wb[3][8], xb[3][3];
#pragma unroll
    for (int s = 0; s < 2; ++s) {
#pragma unroll
      for (int jj = 0; jj < 8; ++jj) wb[s][jj] = bld128(rw, wv, (32 * s + jj) * (HD * 2));
#pragma unroll
      for (int t = 0; t < 3; ++t) xb[s][t] = bld128(rx, xv + s * 64, t * 16 * DM * 2);
    }
#pragma unroll
    for (int s = 0; s < 16; ++s) {
      if (s + 2 < 16) {
#pragma unroll
        for (int jj = 0; jj < 8; ++jj) wb[(s + 2) % 3][jj] = bld128(rw, wv, (32 * (s + 2) + jj) * (HD * 2));
#pragma unroll
        for (int t = 0; t < 3; ++t) xb[(s + 2) % 3][t] = bld128(rx, xv + (s + 2) * 64, t * 16 * DM * 2);
      }
      __builtin_amdgcn_sched_barrier(0);
#pragma unroll
      for (int e = 0; e < 8; ++e) {
        const unsigned sel = (e & 1) ? 0x07060302u : 0x05040100u;
        u32x4 f;
#pragma unroll
        for (int t = 0; t < 4; ++t) f[t] = __builtin_amdgcn_perm(wb[s % 3][2 * t + 1][e >> 1], wb[s % 3][2 * t][e >> 1], sel);
#pragma unroll
        for (int t = 0; t < 3; ++t) acc[t][e] = mfma16(f, xb[s % 3][t], acc[t][e]);
      }
      __builtin_amdgcn_sched_barrier(0);
    }
#pragma unroll
    for (int t = 0; t < 3; ++t)
#pragma unroll
      for (int s = 0; s < 4; ++s)
#pragma unroll
        for (int e = 0; e < 4; ++e) qf[t][s][e] = pack2(acc[t][2 * e][s], acc[t][2 * e + 1][s]);
  }

  int lane2 = lane;
  asm volatile("" : "+v"(lane2));
  const LaneOffs lo = lane_offs(lane2);
  const int g = lane2 >> 4, r = lane2 & 15;
  f32x4 oacc[3][8];
#pragma unroll
  for (int t = 0; t < 3; ++t)
#pragma unroll
    for (int c = 0; c < 8; ++c) oacc[t][c] = f32x4{0.f, 0.f, 0.f, 0.f};
  float mrow[3] = {-__builtin_inff(), -__builtin_inff(), -__builtin_inff()};
  float lrow[3] = {0.f, 0.f, 0.f};
  const unsigned fk = ((r & 3) << 2) | (r >> 2);
  unsigned kra[4];
#pragma unroll
  for (int s = 0; s < 4; ++s) kra[s] = r * 256 + 16 * ((4 * g + s) ^ fk);
  const unsigned vrow = 4 * g + (r >> 2);
  const unsigned vra = 8192 + vrow * 256 + 8 * (r & 1);
  const unsigned vsw = (vrow & 7) << 1, vp = (r >> 1) & 1;
  const unsigned qa = (unsigned)(w & 3);
  const unsigned aok0 = (qa & 1) ? lo.k2 : lo.k0, aok1 = (qa & 1) ? lo.k3 : lo.k1;

  int slot = 0;
  for (int i = 0; i < NBLK; ++i) {
    const unsigned sb = ringoff + slot * BLK_BYTES;
    const int left = NBLK - 1 - i;
    if (roleA) {
      if (left >= 4) WAIT_VM(16); else if (left == 3) WAIT_VM(12); else if (left == 2) WAIT_VM(8); else if (left == 1) WAIT_VM(4); else WAIT_VM(0);
      BAR();
      if (i >= 1 && i + RA - 1 < NBLK) {
        const unsigned m = lds0 + (slot == 0 ? RA - 1 : slot - 1) * BLK_BYTES + qa * 2048;
        dma2(kg + (i + RA - 1) * 8192 + qa * 2048, m, aok0, aok1);
        dma2(vg + (i + RA - 1) * 8192 + qa * 2048, m + 8192, lo.v0, lo.v1);
      }
    } else {
      if (type1 || i < RB) BAR();
      if (left >= 2) WAIT_VM(40); else if (left == 1) WAIT_VM(24); else WAIT_VM(8);
    }
    f32x4 sc[3][2];
#pragma unroll
    for (int tt = 0; tt < 2; ++tt) {
      u32x4 kf[4];
#pragma unroll
      for (int s = 0; s < 4; ++s) kf[s] = *(const LAS u32x4*)((LAS char*)smem + sb + kra[s] + tt * 4096);
#pragma unroll
      for (int t = 0; t < 3; ++t) {
        f32x4 c = f32x4{0.f, 0.f, 0.f, 0.f};
#pragma unroll
        for (int s = 0; s < 4; ++s) c = mfma16(kf[s], qf[t][s], c);
        sc[t][tt] = c;
      }
    }
    if (!roleA) {
      if (i + RB < NBLK) { WAIT_LGKM0(); dma_k_full(kg + (i + RB) * 8192, lds0 + sb, lo); }
      if (left >= 3) WAIT_VM(40); else if (left == 2) WAIT_VM(32); else if (left == 1) WAIT_VM(16); else WAIT_VM(0);
    }
    u32x4 vf[8];
#pragma unroll
    for (int c = 0; c < 8; ++c) {
      const unsigned ad = sb + vra + 16 * ((2 * c + vp) ^ vsw);
      s16x4 l0 = __builtin_amdgcn_ds_read_tr16_b64_v4i16((LAS s16x4*)((LAS char*)smem + ad));
      s16x4 l1 = __builtin_amdgcn_ds_read_tr16_b64_v4i16((LAS s16x4*)((LAS char*)smem + ad + 4096));
      u32x2 u0 = __builtin_bit_cast(u32x2, l0), u1 = __builtin_bit_cast(u32x2, l1);
      vf[c] = u32x4{u0[0], u0[1], u1[0], u1[1]};
    }
    float bm[3];
    bool grow = false;
#pragma unroll
    for (int t = 0; t < 3; ++t) {
      float m0 = fmaxf(fmaxf(sc[t][0][0], sc[t][0][1]), fmaxf(sc[t][0][2], sc[t][0][3]));
      float m1 = fmaxf(fmaxf(sc[t][1][0], sc[t][1][1]), fmaxf(sc[t][1][2], sc[t][1][3]));
      bm[t] = xg_max(fmaxf(m0, m1)) * SCL;
      grow |= !(bm[t] <= mrow[t] + RESCALE_THR);
    }
    if (__any(grow)) {
#pragma unroll
      for (int t = 0; t < 3; ++t) {
        const float mn = fmaxf(mrow[t], bm[t]);
        const float al = __builtin_amdgcn_exp2f(mrow[t] - mn);
        mrow[t] = mn;
        lrow[t] *= al;
#pragma unroll
        for (int c = 0; c < 8; ++c) oacc[t][c] *= al;
      }
    }
    u32x4 pf[3];
#pragma unroll
    for (int t = 0; t < 3; ++t) {
      float pv[8];
#pragma unroll
      for (int e = 0; e < 8; ++e) pv[e] = __builtin_amdgcn_exp2f(fmaf(sc[t][e >> 2][e & 3], SCL, -mrow[t]));
      lrow[t] += ((pv[0] + pv[1]) + (pv[2] + pv[3])) + ((pv[4] + pv[5]) + (pv[6] + pv[7]));
#pragma unroll
      for (int e = 0; e < 4; ++e) pf[t][e] = pack2(pv[2 * e], pv[2 * e + 1]);
    }
    if (!roleA && i + RB < NBLK) { WAIT_LGKM0(); dma_v_full(vg + (i + RB) * 8192, lds0 + sb + 8192, lo); }
#pragma unroll
    for (int c = 0; c < 8; ++c)
#pragma unroll
      for (int t = 0; t < 3; ++t) oacc[t][c] = mfma16(vf[c], pf[t], oacc[t][c]);
    slot = (slot + 1 == rdepth) ? 0 : slot + 1;
  }

  char* op = a.o + ((size_t)(p * 512 + b * 32 + h) * 12) * 1024 + lane2 * 16;
#pragma unroll
  for (int t = 0; t < 3; ++t) {
    const float inv = 1.0f / xg_sum(lrow[t]);
#pragma unroll
    for (int m = 0; m < 4; ++m) {
      u32x4 f;
      f[0] = pack2(oacc[t][2 * m][0] * inv, oacc[t][2 * m][1] * inv);
      f[1] = pack2(oacc[t][2 * m][2] * inv, oacc[t][2 * m][3] * inv);
      f[2] = pack2(oacc[t][2 * m + 1][0] * inv, oacc[t][2 * m + 1][1] * inv);
      f[3] = pack2(oacc[t][2 * m + 1][2] * inv, oacc[t][2 * m + 1][3] * inv);
      *(u32x4*)(op + (t * 4 + m) * 1024) = f;
    }
  }
}

struct OutArgs {
  const char* wo[2];
  const char* o;
  char* out;
};
__global__ __launch_bounds__(512, 2) void oproj_kernel(OutArgs a) {
  extern __shared__ __attribute__((aligned(16))) char smem[];
  const int lane = threadIdx.x & 63;
  const int w = __builtin_amdgcn_readfirstlane(threadIdx.x >> 6);
  const int xcd = blockIdx.x & 7, j = blockIdx.x >> 3;
  const int p = xcd & 1, b = ((xcd >> 1) & 1) * 8 + (j & 7), nb = (xcd >> 2) * 4 + (j >> 3);
  const int g = lane >> 4, r = lane & 15;
  const rsrc_t rw = mkrs(a.wo[p] + w * 512 * (DM * 2), 512 * DM * 2);
  const rsrc_t ro = mkrs(a.o + (size_t)(p * 512 + b * 32 + 4 * w) * 12288, 4 * 12288);
  const unsigned wv = (unsigned)(4 * g * (DM * 2) + nb * 128 + r * 8), ov = (unsigned)(lane * 16);
  f32x4 acc[3][4];
#pragma unroll
  for (int t = 0; t < 3; ++t)
#pragma unroll
    for (int e = 0; e < 4; ++e) acc[t][e] = f32x4{0.f, 0.f, 0.f, 0.f};
  constexpr int PD = 5;
  u32x2 wb[PD + 1][8];
  u32x4 ob[PD + 1][3];
  auto load = [&](int s) {
#pragma unroll
    for (int jj = 0; jj < 8; ++jj) wb[s % (PD + 1)][jj] = bld64(rw, wv + (jj & 3) * (DM * 2), (32 * s + 16 * (jj >> 2)) * (DM * 2));
#pragma unroll
    for (int t = 0; t < 3; ++t) ob[s % (PD + 1)][t] = bld128(ro, ov + (s & 3) * 1024, ((s >> 2) * 12 + t * 4) * 1024);
  };
#pragma unroll
  for (int s = 0; s < PD; ++s) load(s);
#pragma unroll
  for (int s = 0; s < 16; ++s) {
    if (s + PD < 16) load(s + PD);
    __builtin_amdgcn_sched_barrier(0);
#pragma unroll
    for (int e = 0; e < 4; ++e) {
      const unsigned sel = (e & 1) ? 0x07060302u : 0x05040100u;
      u32x4 f;
#pragma unroll
      for (int t = 0; t < 4; ++t) f[t] = __builtin_amdgcn_perm(wb[s % (PD + 1)][2 * t + 1][e >> 1], wb[s % (PD + 1)][2 * t][e >> 1], sel);
#pragma unroll
      for (int t = 0; t < 3; ++t) acc[t][e] = mfma16(f, ob[s % (PD + 1)][t], acc[t][e]);
    }
    __builtin_amdgcn_sched_barrier(0);
  }
  f32x4* red = (f32x4*)smem;
#pragma unroll
  for (int t = 0; t < 3; ++t)
#pragma unroll
    for (int i = 0; i < 4; ++i) red[(w * 12 + t * 4 + i) * 64 + lane] = f32x4{acc[t][0][i], acc[t][1][i], acc[t][2][i], acc[t][3][i]};
  __syncthreads();
  if (w < 6) {
    const int t = w >> 1, ih = w & 1;
    f32x4 s0 = f32x4{0.f, 0.f, 0.f, 0.f}, s1 = s0;
#pragma unroll
    for (int ww = 0; ww < 8; ++ww) {
      s0 += red[(ww * 12 + t * 4 + 2 * ih) * 64 + lane];
      s1 += red[(ww * 12 + t * 4 + 2 * ih + 1) * 64 + lane];
    }
    u32x4 f = {pack2(s0[0], s0[1]), pack2(s0[2], s0[3]), pack2(s1[0], s1[1]), pack2(s1[2], s1[3])};
    *(u32x4*)(a.out + ((size_t)(b * SQ + 16 * t + r) * 1024 + p * 512 + nb * 64 + 16 * g + 8 * ih) * 2) = f;
  }
}

extern "C" void kernel_launch(void* const* d_in, const int* in_sizes, int n_in, void* d_out, int out_size, void* d_ws,
                              size_t ws_size, hipStream_t stream) {
  (void)in_sizes; (void)n_in; (void)out_size; (void)ws_size;
  AttnArgs a;
  a.x[0] = (const char*)d_in[0]; a.k[0] = (const char*)d_in[1]; a.v[0] = (const char*)d_in[2]; a.wq[0] = (const char*)d_in[3];
  a.x[1] = (const char*)d_in[5]; a.k[1] = (const char*)d_in[6]; a.v[1] = (const char*)d_in[7]; a.wq[1] = (const char*)d_in[8];
  a.o = (char*)d_ws;
  OutArgs oa;
  oa.wo[0] = (const char*)d_in[4]; oa.wo[1] = (const char*)d_in[9]; oa.o = (const char*)d_ws; oa.out = (char*)d_out;
  (void)hipFuncSetAttribute((const void*)attn_kernel, hipFuncAttributeMaxDynamicSharedMemorySize, LDS_ATTN);
  (void)hipFuncSetAttribute((const void*)oproj_kernel, hipFuncAttributeMaxDynamicSharedMemorySize, 98304);
  hipLaunchKernelGGL(attn_kernel, dim3(256), dim3(512), LDS_ATTN, stream, a);
  hipLaunchKernelGGL(oproj_kernel, dim3(256), dim3(512), 98304, stream, oa);
}
```

```cpp
#include <hip/hip_runtime.h>
#include <hip/hip_bf16.h>
#include <stdint.h>

typedef __attribute__((ext_vector_type(8))) __bf16 bf16x8;
typedef __attribute__((ext_vector_type(2))) __bf16 bf16x2;
typedef __attribute__((ext_vector_type(4))) short s16x4;
typedef __attribute__((ext_vector_type(4))) float f32x4;
typedef __attribute__((ext_vector_type(2))) float f32x2;
typedef __attribute__((ext_vector_type(4))) unsigned u32x4;
typedef __attribute__((ext_vector_type(2))) unsigned u32x2;
#define LAS __attribute__((address_space(3)))

#define KV_NT 1
#define NT_STR " nt"

constexpr int SQ = 48, DM = 512, NH = 32, SK = 1024, DH = 128, HD = NH * DH;
constexpr int NBLK = SK / 32;
constexpr int BLK_BYTES = 16384;
constexpr int LDS_ATTN = 10 * BLK_BYTES;
constexpr int XOFF1 = 7 * BLK_BYTES, XOFF2 = 6 * BLK_BYTES;
#define FILL_STEP 13
#define QSTAMP(n)
#define FILL1_LATE 0
#define NO_WQ_PF 0
constexpr float SCL = 0.088388347648318447f * 1.4426950408889634f;
constexpr float RESCALE_THR = 6.0f;

__device__ __forceinline__ unsigned pack2(float lo, float hi) {
  f32x2 f = {lo, hi};
  return __builtin_bit_cast(unsigned, __builtin_convertvector(f, bf16x2));
}
__device__ __forceinline__ f32x4 mfma16(u32x4 a, u32x4 b, f32x4 c) {
  return __builtin_amdgcn_mfma_f32_16x16x32_bf16(__builtin_bit_cast(bf16x8, a), __builtin_bit_cast(bf16x8, b), c, 0, 0, 0);
}
__device__ __forceinline__ float xg_max(float v) {
  auto r = __builtin_amdgcn_permlane16_swap(__float_as_uint(v), __float_as_uint(v), false, false);
  v = fmaxf(__uint_as_float(r[0]), __uint_as_float(r[1]));
  auto s = __builtin_amdgcn_permlane32_swap(__float_as_uint(v), __float_as_uint(v), false, false);
  return fmaxf(__uint_as_float(s[0]), __uint_as_float(s[1]));
}
__device__ __forceinline__ float xg_sum(float v) {
  auto r = __builtin_amdgcn_permlane16_swap(__float_as_uint(v), __float_as_uint(v), false, false);
  v = __uint_as_float(r[0]) + __uint_as_float(r[1]);
  auto s = __builtin_amdgcn_permlane32_swap(__float_as_uint(v), __float_as_uint(v), false, false);
  return __uint_as_float(s[0]) + __uint_as_float(s[1]);
}

#define DMA4_BODY(POL)                                                        \
  unsigned keep;                                                              \
  asm volatile(                                                               \
      "s_nop 4\n\ts_mov_b32 %0, m0\n\ts_mov_b32 m0, %1\n\ts_nop 0\n\t"      \
      "global_load_lds_dwordx4 %3, %2" POL "\n\t"                             \
      "global_load_lds_dwordx4 %4, %2 offset:1024" POL "\n\t"                 \
      "global_load_lds_dwordx4 %5, %2 offset:2048" POL "\n\t"                 \
      "global_load_lds_dwordx4 %6, %2 offset:3072" POL "\n\t"                 \
      "s_mov_b32 m0, %0"                                                      \
      : "=&s"(keep)                                                           \
      : "s"(m), "s"(g), "v"(o0), "v"(o1), "v"(o2), "v"(o3)                    \
      : "memory")
__device__ __forceinline__ void dma4(const char* g, unsigned m, unsigned o0, unsigned o1, unsigned o2, unsigned o3) { DMA4_BODY(NT_STR); }
__device__ __forceinline__ void dma4c(const char* g, unsigned m, unsigned o0, unsigned o1, unsigned o2, unsigned o3) { DMA4_BODY(""); }
#define WAIT_VM(n) asm volatile("s_waitcnt vmcnt(" #n ")" ::: "memory")
#define WAIT_LGKM0() asm volatile("s_waitcnt lgkmcnt(0)" ::: "memory")
#define BAR() asm volatile("s_barrier" ::: "memory")

typedef __amdgpu_buffer_rsrc_t rsrc_t;
__device__ __forceinline__ rsrc_t mkrs(const void* p, int bytes) { return __builtin_amdgcn_make_buffer_rsrc((void*)p, (short)0, bytes, 0x00020000); }
__device__ __forceinline__ u32x4 bld128(rsrc_t rs, unsigned vo, unsigned so) { return __builtin_amdgcn_raw_buffer_load_b128(rs, vo, so, 0); }
__device__ __forceinline__ u32x2 bld64(rsrc_t rs, unsigned vo, unsigned so) { return __builtin_amdgcn_raw_buffer_load_b64(rs, vo, so, 0); }

struct AttnArgs {
  const char* x[2];
  const char* k[2];
  const char* v[2];
  const char* wq[2];
  char* o;
};

template <bool XLDS>
__device__ __forceinline__ void qproj(u32x4 (&qf)[3][4], const char* wq, int wbytes, const char* x, char* smem, unsigned xoff, int lane) {
  f32x4 acc[3][8];
#pragma unroll
  for (int t = 0; t < 3; ++t)
#pragma unroll
    for (int e = 0; e < 8; ++e) acc[t][e] = f32x4{0.f, 0.f, 0.f, 0.f};
  const rsrc_t rw = mkrs(wq, wbytes);
  const rsrc_t rx = mkrs(x, SQ * DM * 2);
  const unsigned g = lane >> 4, r = lane & 15;
  const unsigned wv = g * (32 * HD * 2) + r * 16;
  const unsigned xv = r * (DM * 2) + g * 64;
  const unsigned xl = xoff + r * (DM * 2) + 16 * ((4 * g) ^ (((r & 3) << 2) | (r >> 2)));
  constexpr int XA = XLDS ? 1 : 2;
  u32x4 wb[3][8], xb[XA + 1][3];
  auto loadw = [&](int s) {
#pragma unroll
    for (int jj = 0; jj < 8; ++jj) wb[s % 3][jj] = bld128(rw, wv, (128 * (s >> 2) + 8 * (s & 3) + jj) * (HD * 2));
  };
  auto loadx = [&](int s) {
#pragma unroll
    for (int t = 0; t < 3; ++t) {
      if (XLDS) xb[s % (XA + 1)][t] = *(const LAS u32x4*)((LAS char*)smem + (xl ^ (16 * (s & 3))) + t * 16 * DM * 2 + (s >> 2) * 256);
      else xb[s % (XA + 1)][t] = bld128(rx, xv + (s >> 2) * 256 + (s & 3) * 16, t * 16 * DM * 2);
    }
  };
  loadw(0);
  if (!XLDS) loadx(0);
  loadw(1);
  if (!XLDS) loadx(1);
  BAR();
  QSTAMP(0);
  if (XLDS) loadx(0);
#pragma unroll
  for (int s = 0; s < 16; ++s) {
    if (s + 2 < 16) loadw(s + 2);
    if (s + XA < 16) loadx(s + XA);
    if (s == FILL_STEP) BAR();
    __builtin_amdgcn_sched_barrier(0);
#pragma unroll
    for (int e = 0; e < 8; ++e) {
      const unsigned sel = (e & 1) ? 0x07060302u : 0x05040100u;
      u32x4 f;
#pragma unroll
      for (int t = 0; t < 4; ++t) f[t] = __builtin_amdgcn_perm(wb[s % 3][2 * t + 1][e >> 1], wb[s % 3][2 * t][e >> 1], sel);
#pragma unroll
      for (int t = 0; t < 3; ++t) acc[t][e] = mfma16(f, xb[s % (XA + 1)][t], acc[t][e]);
    }
    __builtin_amdgcn_sched_barrier(0);
    if (s == 0) QSTAMP(1);
    if (s == 7) QSTAMP(2);
  }
#pragma unroll
  for (int t = 0; t < 3; ++t)
#pragma unroll
    for (int s = 0; s < 4; ++s)
#pragma unroll
      for (int e = 0; e < 4; ++e) qf[t][s][e] = pack2(acc[t][2 * e][s], acc[t][2 * e + 1][s]);
}

__global__ __launch_bounds__(512, 2) void attn_kernel(AttnArgs a) {
  extern __shared__ __attribute__((aligned(16))) char smem[];
  const int lane = threadIdx.x & 63;
  const int w = __builtin_amdgcn_readfirstlane(threadIdx.x >> 6);
  const int xcd = blockIdx.x & 7, j = blockIdx.x >> 3;
  const bool type1 = j < 16;
  const int b = j & 15;
  const unsigned lds0 = (unsigned)(uintptr_t)(LAS char*)smem;
  const int nst = type1 ? 2 : 3, rdepth = type1 ? 5 : 3;
  const unsigned xoff = type1 ? XOFF1 : XOFF2;

  if (w >= (type1 ? 5 : 4)) {
    const unsigned g = lane >> 4, r = lane & 15;
    unsigned t0 = 0, t1 = 0, t2 = 0, t3 = 0;
    if (w == 7 && !NO_WQ_PF) {
      const unsigned woff = (unsigned)((16 * j + (lane >> 3)) * (HD * 2) + xcd * 1024 + (lane & 7) * 128);
      asm volatile(
          "s_nop 4\n\t"
          "global_load_dword %0, %4, %6\n\tglobal_load_dword %1, %5, %6\n\t"
          "global_load_dword %2, %4, %7\n\tglobal_load_dword %3, %5, %7"
          : "=&v"(t0), "=&v"(t1), "=&v"(t2), "=&v"(t3)
          : "v"(woff), "v"(woff + 8u * HD * 2), "s"(a.wq[0]), "s"(a.wq[1])
          : "memory");
    }
    const int li = 7 - w;
    {
      const char* xg = a.x[type1 ? 0 : 1] + (size_t)b * (SQ * DM * 2);
      const int nq = type1 ? 4 : 3;
      for (int q = 0; q < nq; ++q) {
        const int rg = li * nq + q;
        const unsigned base = g * 256 + 16 * (r ^ (unsigned)(rg & 3));
        dma4c(xg + rg * 4096, lds0 + xoff + rg * 4096, base, base ^ 64u, base ^ 128u, base ^ 192u);
      }
    }
    const int nu = (type1 && li == 2) ? 2 : 3;
    const char* up[3];
    unsigned ul[3], uo[3][4];
    bool uA[3];
#pragma unroll
    for (int u = 0; u < 3; ++u) {
      const int id = (li * 3 + u) & (type1 ? 7 : 15);
      const int st = id >> 2, isv = (id >> 1) & 1, hf = id & 1;
      uA[u] = type1 && st == 1;
      const size_t sidx = uA[u] ? (size_t)(b * 8 + xcd) : (size_t)(b * 32 + 4 * xcd + (type1 ? 0 : 1 + st));
      const char* kb = uA[u] ? a.k[0] : a.k[1];
      const char* vb = uA[u] ? a.v[0] : a.v[1];
      up[u] = (isv ? vb : kb) + sidx * (SK * DH * 2) + hf * 4096;
      ul[u] = lds0 + st * BLK_BYTES + isv * 8192 + hf * 4096;
#pragma unroll
      for (int q = 0; q < 4; ++q) uo[u][q] = g * 256 + 16 * (r ^ (isv ? (((q & 1) * 4 + g) << 1) : ((g << 2) | q)));
    }
    const unsigned sstride = nst * BLK_BYTES;
    auto issue = [&](int blk, int slot, int which) {
#pragma unroll
      for (int u = 0; u < 3; ++u)
        if (u < nu && (which == 0 || (which == 2) == uA[u])) dma4(up[u] + blk * 8192, ul[u] + slot * sstride, uo[u][0], uo[u][1], uo[u][2], uo[u][3]);
    };
    if (type1) {
      issue(0, 0, 0); issue(1, 1, 0); issue(2, 2, 0); issue(3, 3, 1);
      if (li == 0) WAIT_VM(48); else if (li == 1) WAIT_VM(40); else WAIT_VM(24);
      BAR();
      BAR();
      WAIT_VM(0);
    } else {
      WAIT_VM(0);
      BAR();
      BAR();
      issue(0, 0, 0); issue(1, 1, 0);
    }
    int slot = rdepth - 1;
    for (int i = 0; i < NBLK; ++i) {
      const int left = NBLK - 1 - i;
      if (!type1) { if (left >= 1) WAIT_VM(12); else WAIT_VM(0); }
      else if (nu == 3) { if (left >= 3) WAIT_VM(36); else if (left == 2) WAIT_VM(24); else if (left == 1) WAIT_VM(12); else WAIT_VM(0); }
      else { if (left >= 3) WAIT_VM(24); else if (left == 2) WAIT_VM(16); else if (left == 1) WAIT_VM(8); else WAIT_VM(0); }
      BAR();
      if (type1 && i == 0) issue(3, 3, 2);
      if (i + rdepth - 1 < NBLK) issue(i + rdepth - 1, slot, 0);
      slot = (slot + 1 == rdepth) ? 0 : slot + 1;
    }
    asm volatile("" ::"v"(t0), "v"(t1), "v"(t2), "v"(t3));
    return;
  }
  if (!type1 && w == 3) return;

  const bool roleA = type1 && w < 4;
  const int p = roleA ? 0 : 1;
  const int h = roleA ? 4 * xcd + w : (type1 ? 4 * xcd : 4 * xcd + 1 + w);
  const unsigned ringoff = (unsigned)((type1 ? (roleA ? 1 : 0) : w) * BLK_BYTES);
  const bool xlds = !(type1 && w == 4);

  u32x4 qf[3][4];
  if (xlds) qproj<true>(qf, a.wq[p] + h * 256, DM * HD * 2 - h * 256, a.x[p] + b * (SQ * DM * 2), smem, xoff, lane);
  else qproj<false>(qf, a.wq[p] + h * 256, DM * HD * 2 - h * 256, a.x[p] + b * (SQ * DM * 2), smem, xoff, lane);

  int lane2 = lane;
  asm volatile("" : "+v"(lane2));
  const int g = lane2 >> 4, r = lane2 & 15;
  f32x4 oacc[3][8];
#pragma unroll
  for (int t = 0; t < 3; ++t)
#pragma unroll
    for (int c = 0; c < 8; ++c) oacc[t][c] = f32x4{0.f, 0.f, 0.f, 0.f};
  float mrow[3] = {-__builtin_inff(), -__builtin_inff(), -__builtin_inff()};
  float lrow[3] = {0.f, 0.f, 0.f};
  const unsigned fk = ((r & 3) << 2) | (r >> 2);
  unsigned kra[4];
#pragma unroll
  for (int s = 0; s < 4; ++s) kra[s] = r * 256 + 16 * ((4 * g + s) ^ fk);
  const unsigned vrow = 4 * g + (r >> 2);
  const unsigned vra = 8192 + vrow * 256 + 8 * (r & 1);
  const unsigned vsw = (vrow & 7) << 1, vp = (r >> 1) & 1;

  int slot = 0;
  for (int i = 0; i < NBLK; ++i) {
    const unsigned sb = ringoff + slot * nst * BLK_BYTES;
    WAIT_LGKM0();
    BAR();
    f32x4 sc[3][2];
#pragma unroll
    for (int tt = 0; tt < 2; ++tt) {
      u32x4 kf[4];
#pragma unroll
      for (int s = 0; s < 4; ++s) kf[s] = *(const LAS u32x4*)((LAS char*)smem + sb + kra[s] + tt * 4096);
#pragma unroll
      for (int t = 0; t < 3; ++t) {
        f32x4 c = f32x4{0.f, 0.f, 0.f, 0.f};
#pragma unroll
        for (int s = 0; s < 4; ++s) c = mfma16(kf[s], qf[t][s], c);
        sc[t][tt] = c;
      }
    }
    u32x4 vf[8];
#pragma unroll
    for (int c = 0; c < 8; ++c) {
      const unsigned ad = sb + vra + 16 * ((2 * c + vp) ^ vsw);
      s16x4 l0 = __builtin_amdgcn_ds_read_tr16_b64_v4i16((LAS s16x4*)((LAS char*)smem + ad));
      s16x4 l1 = __builtin_amdgcn_ds_read_tr16_b64_v4i16((LAS s16x4*)((LAS char*)smem + ad + 4096));
      u32x2 u0 = __builtin_bit_cast(u32x2, l0), u1 = __builtin_bit_cast(u32x2, l1);
      vf[c] = u32x4{u0[0], u0[1], u1[0], u1[1]};
    }
    float bm[3];
    bool grow = false;
#pragma unroll
    for (int t = 0; t < 3; ++t) {
      float m0 = fmaxf(fmaxf(sc[t][0][0], sc[t][0][1]), fmaxf(sc[t][0][2], sc[t][0][3]));
      float m1 = fmaxf(fmaxf(sc[t][1][0], sc[t][1][1]), fmaxf(sc[t][1][2], sc[t][1][3]));
      bm[t] = xg_max(fmaxf(m0, m1)) * SCL;
      grow |= !(bm[t] <= mrow[t] + RESCALE_THR);
    }
    if (__any(grow)) {
#pragma unroll
      for (int t = 0; t < 3; ++t) {
        const float mn = fmaxf(mrow[t], bm[t]);
        const float al = __builtin_amdgcn_exp2f(mrow[t] - mn);
        mrow[t] = mn;
        lrow[t] *= al;
#pragma unroll
        for (int c = 0; c < 8; ++c) oacc[t][c] *= al;
      }
    }
    u32x4 pf[3];
#pragma unroll
    for (int t = 0; t < 3; ++t) {
      float pv[8];
#pragma unroll
      for (int e = 0; e < 8; ++e) pv[e] = __builtin_amdgcn_exp2f(fmaf(sc[t][e >> 2][e & 3], SCL, -mrow[t]));
      lrow[t] += ((pv[0] + pv[1]) + (pv[2] + pv[3])) + ((pv[4] + pv[5]) + (pv[6] + pv[7]));
#pragma unroll
      for (int e = 0; e < 4; ++e) pf[t][e] = pack2(pv[2 * e], pv[2 * e + 1]);
    }
#pragma unroll
    for (int c = 0; c < 8; ++c)
#pragma unroll
      for (int t = 0; t < 3; ++t) oacc[t][c] = mfma16(vf[c], pf[t], oacc[t][c]);
    slot = (slot + 1 == rdepth) ? 0 : slot + 1;
  }

  char* op = a.o + ((size_t)(p * 512 + b * 32 + h) * 12) * 1024 + lane2 * 16;
#pragma unroll
  for (int t = 0; t < 3; ++t) {
    const float inv = 1.0f / xg_sum(lrow[t]);
#pragma unroll
    for (int m = 0; m < 4; ++m) {
      u32x4 f;
      f[0] = pack2(oacc[t][2 * m][0] * inv, oacc[t][2 * m][1] * inv);
      f[1] = pack2(oacc[t][2 * m][2] * inv, oacc[t][2 * m][3] * inv);
      f[2] = pack2(oacc[t][2 * m + 1][0] * inv, oacc[t][2 * m + 1][1] * inv);
      f[3] = pack2(oacc[t][2 * m + 1][2] * inv, oacc[t][2 * m + 1][3] * inv);
      *(u32x4*)(op + (t * 4 + m) * 1024) = f;
    }
  }
}

struct OutArgs {
  const char* wo[2];
  const char* o;
  char* out;
};
__global__ __launch_bounds__(512, 2) void oproj_kernel(OutArgs a) {
  extern __shared__ __attribute__((aligned(16))) char smem[];
  const int lane = threadIdx.x & 63;
  const int w = __builtin_amdgcn_readfirstlane(threadIdx.x >> 6);
  const int xcd = blockIdx.x & 7, j = blockIdx.x >> 3;
  const int p = xcd & 1, b = ((xcd >> 1) & 1) * 8 + (j & 7), nb = (xcd >> 2) * 4 + (j >> 3);
  const int g = lane >> 4, r = lane & 15;
  const rsrc_t rw = mkrs(a.wo[p] + w * 512 * (DM * 2), 512 * DM * 2);
  const rsrc_t ro = mkrs(a.o + (size_t)(p * 512 + b * 32 + 4 * w) * 12288, 4 * 12288);
  const unsigned wv = (unsigned)(4 * g * (DM * 2) + nb * 128 + r * 8), ov = (unsigned)(lane * 16);
  f32x4 acc[3][4];
#pragma unroll
  for (int t = 0; t < 3; ++t)
#pragma unroll
    for (int e = 0; e < 4; ++e) acc[t][e] = f32x4{0.f, 0.f, 0.f, 0.f};
  constexpr int PD = 5;
  u32x2 wb[PD + 1][8];
  u32x4 ob[PD + 1][3];
  auto load = [&](int s) {
#pragma unroll
    for (int jj = 0; jj < 8; ++jj) wb[s % (PD + 1)][jj] = bld64(rw, wv + (jj & 3) * (DM * 2), (32 * s + 16 * (jj >> 2)) * (DM * 2));
#pragma unroll
    for (int t = 0; t < 3; ++t) ob[s % (PD + 1)][t] = bld128(ro, ov + (s & 3) * 1024, ((s >> 2) * 12 + t * 4) * 1024);
  };
#pragma unroll
  for (int s = 0; s < PD; ++s) load(s);
#pragma unroll
  for (int s = 0; s < 16; ++s) {
    if (s + PD < 16) load(s + PD);
    __builtin_amdgcn_sched_barrier(0);
#pragma unroll
    for (int e = 0; e < 4; ++e) {
      const unsigned sel = (e & 1) ? 0x07060302u : 0x05040100u;
      u32x4 f;
#pragma unroll
      for (int t = 0; t < 4; ++t) f[t] = __builtin_amdgcn_perm(wb[s % (PD + 1)][2 * t + 1][e >> 1], wb[s % (PD + 1)][2 * t][e >> 1], sel);
#pragma unroll
      for (int t = 0; t < 3; ++t) acc[t][e] = mfma16(f, ob[s % (PD + 1)][t], acc[t][e]);
    }
    __builtin_amdgcn_sched_barrier(0);
  }
  f32x4* red = (f32x4*)smem;
#pragma unroll
  for (int t = 0; t < 3; ++t)
#pragma unroll
    for (int i = 0; i < 4; ++i) red[(w * 12 + t * 4 + i) * 64 + lane] = f32x4{acc[t][0][i], acc[t][1][i], acc[t][2][i], acc[t][3][i]};
  __syncthreads();
  if (w < 6) {
    const int t = w >> 1, ih = w & 1;
    f32x4 s0 = f32x4{0.f, 0.f, 0.f, 0.f}, s1 = s0;
#pragma unroll
    for (int ww = 0; ww < 8; ++ww) {
      s0 += red[(ww * 12 + t * 4 + 2 * ih) * 64 + lane];
      s1 += red[(ww * 12 + t * 4 + 2 * ih + 1) * 64 + lane];
    }
    u32x4 f = {pack2(s0[0], s0[1]), pack2(s0[2], s0[3]), pack2(s1[0], s1[1]), pack2(s1[2], s1[3])};
    *(u32x4*)(a.out + ((size_t)(b * SQ + 16 * t + r) * 1024 + p * 512 + nb * 64 + 16 * g + 8 * ih) * 2) = f;
  }
}

extern "C" void kernel_launch(void* const* d_in, const int* in_sizes, int n_in, void* d_out, int out_size, void* d_ws,
                              size_t ws_size, hipStream_t stream) {
  (void)in_sizes; (void)n_in; (void)out_size; (void)ws_size;
  AttnArgs a;
  a.x[0] = (const char*)d_in[0]; a.k[0] = (const char*)d_in[1]; a.v[0] = (const char*)d_in[2]; a.wq[0] = (const char*)d_in[3];
  a.x[1] = (const char*)d_in[5]; a.k[1] = (const char*)d_in[6]; a.v[1] = (const char*)d_in[7]; a.wq[1] = (const char*)d_in[8];
  a.o = (char*)d_ws;
  OutArgs oa;
  oa.wo[0] = (const char*)d_in[4]; oa.wo[1] = (const char*)d_in[9]; oa.o = (const char*)d_ws; oa.out = (char*)d_out;
  (void)hipFuncSetAttribute((const void*)attn_kernel, hipFuncAttributeMaxDynamicSharedMemorySize, LDS_ATTN);
  (void)hipFuncSetAttribute((const void*)oproj_kernel, hipFuncAttributeMaxDynamicSharedMemorySize, 98304);
  hipLaunchKernelGGL(attn_kernel, dim3(256), dim3(512), LDS_ATTN, stream, a);
  hipLaunchKernelGGL(oproj_kernel, dim3(256), dim3(512), 98304, stream, oa);
}
```

```cpp
#include <hip/hip_runtime.h>
#include <hip/hip_bf16.h>
#include <stdint.h>

typedef __attribute__((ext_vector_type(8))) __bf16 bf16x8;
typedef __attribute__((ext_vector_type(2))) __bf16 bf16x2;
typedef __attribute__((ext_vector_type(4))) short s16x4;
typedef __attribute__((ext_vector_type(4))) float f32x4;
typedef __attribute__((ext_vector_type(2))) float f32x2;
typedef __attribute__((ext_vector_type(4))) unsigned u32x4;
typedef __attribute__((ext_vector_type(2))) unsigned u32x2;
#define LAS __attribute__((address_space(3)))

#define KV_NT 1
#define NT_STR " nt"

constexpr int SQ = 48, DM = 512, NH = 32, SK = 1024, DH = 128, HD = NH * DH;
constexpr int NBLK = SK / 32;
constexpr int BLK_BYTES = 16384;
constexpr int LDS_ATTN = 10 * BLK_BYTES;
constexpr int XOFF1 = 7 * BLK_BYTES, XOFF2 = 6 * BLK_BYTES;
#define FILL_STEP 13
#define QSTAMP(n)
#define FILL1_LATE 0
#define NO_WQ_PF 0
#define WO_PF_IT 31
#define WO_GRAN 128
#define WO_PER 64
#define X_NT 0
#define WQ_AUX 0
#define WQPF_AUX 0
#define TOUCH 1
#define NRB 4
#define RB0 5
#define RBS 5
#define KPH 0
#define WO_MATCH 1
#define WQ_PF3 2
constexpr float SCL = 0.088388347648318447f * 1.4426950408889634f;
constexpr float RESCALE_THR = 6.0f;

__device__ __forceinline__ unsigned pack2(float lo, float hi) {
  f32x2 f = {lo, hi};
  return __builtin_bit_cast(unsigned, __builtin_convertvector(f, bf16x2));
}
__device__ __forceinline__ f32x4 mfma16(u32x4 a, u32x4 b, f32x4 c) {
  return __builtin_amdgcn_mfma_f32_16x16x32_bf16(__builtin_bit_cast(bf16x8, a), __builtin_bit_cast(bf16x8, b), c, 0, 0, 0);
}
__device__ __forceinline__ float xg_max(float v) {
  auto r = __builtin_amdgcn_permlane16_swap(__float_as_uint(v), __float_as_uint(v), false, false);
  v = fmaxf(__uint_as_float(r[0]), __uint_as_float(r[1]));
  auto s = __builtin_amdgcn_permlane32_swap(__float_as_uint(v), __float_as_uint(v), false, false);
  return fmaxf(__uint_as_float(s[0]), __uint_as_float(s[1]));
}
__device__ __forceinline__ float max3(float a, float b, float c) {
  return __builtin_elementwise_maximum(__builtin_elementwise_maximum(a, b), c);
}
#define SB() __builtin_amdgcn_sched_barrier(0)

#define DMA4_BODY(POL)                                                        \
  unsigned keep;                                                              \
  asm volatile(                                                               \
      "s_nop 4\n\ts_mov_b32 %0, m0\n\ts_mov_b32 m0, %1\n\ts_nop 0\n\t"      \
      "global_load_lds_dwordx4 %3, %2" POL "\n\t"                             \
      "global_load_lds_dwordx4 %4, %2 offset:1024" POL "\n\t"                 \
      "global_load_lds_dwordx4 %5, %2 offset:2048" POL "\n\t"                 \
      "global_load_lds_dwordx4 %6, %2 offset:3072" POL "\n\t"                 \
      "s_mov_b32 m0, %0"                                                      \
      : "=&s"(keep)                                                           \
      : "s"(m), "s"(g), "v"(o0), "v"(o1), "v"(o2), "v"(o3)                    \
      : "memory")
__device__ __forceinline__ void dma4(const char* g, unsigned m, unsigned o0, unsigned o1, unsigned o2, unsigned o3) { DMA4_BODY(NT_STR); }
__device__ __forceinline__ void dma4c(const char* g, unsigned m, unsigned o0, unsigned o1, unsigned o2, unsigned o3) {
  DMA4_BODY("");
}
#define WAIT_VM(n) asm volatile("s_waitcnt vmcnt(" #n ")" ::: "memory")
#define WAIT_LGKM0() asm volatile("s_waitcnt lgkmcnt(0)" ::: "memory")
#define BAR() asm volatile("s_barrier" ::: "memory")

typedef __amdgpu_buffer_rsrc_t rsrc_t;
__device__ __forceinline__ rsrc_t mkrs(const void* p, int bytes) { return __builtin_amdgcn_make_buffer_rsrc((void*)p, (short)0, bytes, 0x00020000); }
__device__ __forceinline__ u32x4 bld128(rsrc_t rs, unsigned vo, unsigned so) { return __builtin_amdgcn_raw_buffer_load_b128(rs, vo, so, 0); }
__device__ __forceinline__ u32x2 bld64(rsrc_t rs, unsigned vo, unsigned so) { return __builtin_amdgcn_raw_buffer_load_b64(rs, vo, so, 0); }

struct AttnArgs {
  const char* x[2];
  const char* k[2];
  const char* v[2];
  const char* wq[2];
  const char* wo[2];
  char* o;
};

template <bool XLDS>
__device__ __forceinline__ void qproj(u32x4 (&qf)[3][4], const char* wq, int wbytes, const char* x, char* smem, unsigned xoff, int lane) {
  f32x4 acc[3][8];
#pragma unroll
  for (int t = 0; t < 3; ++t)
#pragma unroll
    for (int e = 0; e < 8; ++e) acc[t][e] = f32x4{0.f, 0.f, 0.f, 0.f};
  const rsrc_t rw = mkrs(wq, wbytes);
  const rsrc_t rx = mkrs(x, SQ * DM * 2);
  const unsigned g = lane >> 4, r = lane & 15;
  const unsigned wv = g * (32 * HD * 2) + r * 16;
  const unsigned xv = r * (DM * 2) + g * 64;
  const unsigned xl = xoff + r * (DM * 2) + 16 * ((4 * g) ^ (((r & 3) << 2) | (r >> 2)));
  constexpr int XA = XLDS ? 1 : 2;
  u32x4 wb[3][8], xb[XA + 1][3];
  auto loadw = [&](int s, int jj) { wb[s % 3][jj] = bld128(rw, wv, (128 * (s >> 2) + 8 * (s & 3) + jj) * (HD * 2)); };
  auto loadx = [&](int s, int t) {
    if (XLDS) xb[s % (XA + 1)][t] = *(const LAS u32x4*)((LAS char*)smem + (xl ^ (16 * (s & 3))) + t * 16 * DM * 2 + (s >> 2) * 256);
    else xb[s % (XA + 1)][t] = bld128(rx, xv + (s >> 2) * 256 + (s & 3) * 16, t * 16 * DM * 2);
  };
  u32x4 f[2];
  auto tr = [&](int n, int t) {
    const int s = n >> 3, e = n & 7;
    f[n & 1][t] = __builtin_amdgcn_perm(wb[s % 3][2 * t + 1][e >> 1], wb[s % 3][2 * t][e >> 1], (e & 1) ? 0x07060302u : 0x05040100u);
  };
#pragma unroll
  for (int s = 0; s < 2; ++s) {
#pragma unroll
    for (int jj = 0; jj < 8; ++jj) loadw(s, jj);
    if (!XLDS)
#pragma unroll
      for (int t = 0; t < 3; ++t) loadx(s, t);
  }
  BAR();
  QSTAMP(0);
  if (XLDS)
#pragma unroll
    for (int t = 0; t < 3; ++t) loadx(0, t);
#pragma unroll
  for (int t = 0; t < 4; ++t) tr(0, t);
#pragma unroll
  for (int s = 0; s < 16; ++s)
#pragma unroll
  for (int e = 0; e < 8; ++e) {
    const int n = 8 * s + e;
    if (n == 8 * FILL_STEP + 4) BAR();
    SB();
    acc[0][e] = mfma16(f[n & 1], xb[s % (XA + 1)][0], acc[0][e]);
    if (n + 1 < 128) { tr(n + 1, 0); tr(n + 1, 1); }
    SB();
    acc[1][e] = mfma16(f[n & 1], xb[s % (XA + 1)][1], acc[1][e]);
    if (n + 1 < 128) { tr(n + 1, 2); tr(n + 1, 3); }
    SB();
    acc[2][e] = mfma16(f[n & 1], xb[s % (XA + 1)][2], acc[2][e]);
    if (e < 4 && s + 2 < 16) { loadw(s + 2, 2 * e); loadw(s + 2, 2 * e + 1); }
    if (e >= 4 && e < 7 && s + XA < 16) loadx(s + XA, e - 4);
    if (n == 7) QSTAMP(1);
    if (n == 63) QSTAMP(2);
  }
  SB();
#pragma unroll
  for (int t = 0; t < 3; ++t)
#pragma unroll
    for (int s = 0; s < 4; ++s)
#pragma unroll
      for (int e = 0; e < 4; ++e) qf[t][s][e] = pack2(acc[t][2 * e][s], acc[t][2 * e + 1][s]);
}

__global__ __launch_bounds__(512, 2) void attn_kernel(AttnArgs a) {
  extern __shared__ __attribute__((aligned(16))) char smem[];
  const int lane = threadIdx.x & 63;
  const int w = __builtin_amdgcn_readfirstlane(threadIdx.x >> 6);
  const int xcd = blockIdx.x & 7, j = blockIdx.x >> 3;
  const bool type1 = j < 16;
  const int b = j & 15;
  const unsigned lds0 = (unsigned)(uintptr_t)(LAS char*)smem;
  const int nst = type1 ? 2 : 3, rdepth = type1 ? 5 : 3;
  const unsigned xoff = type1 ? XOFF1 : XOFF2;

  if (w >= (type1 ? 5 : 4)) {
    const unsigned g = lane >> 4, r = lane & 15;
    unsigned t0 = 0, t1 = 0, t2 = 0, t3 = 0;
    unsigned tq[8];
#pragma unroll
    for (int q = 0; q < 8; ++q) tq[q] = 0;
    if (WQ_PF3 == 2 && type1 && w == 5) {
      const unsigned vo = (unsigned)((32 * b + (lane >> 3)) * (HD * 2) + xcd * 1024 + (lane & 7) * 128);
      const unsigned v1 = vo + 8u * HD * 2, v2 = vo + 16u * HD * 2, v3 = vo + 24u * HD * 2;
      asm volatile(
          "s_nop 4\n\t"
          "global_load_dword %0, %8, %13\n\tglobal_load_dword %1, %9, %13\n\tglobal_load_dword %2, %10, %13\n\tglobal_load_dword %3, %11, %13\n\t"
          "global_load_dword %4, %8, %12\n\tglobal_load_dword %5, %9, %12\n\tglobal_load_dword %6, %10, %12\n\tglobal_load_dword %7, %11, %12"
          : "=&v"(tq[0]), "=&v"(tq[1]), "=&v"(tq[2]), "=&v"(tq[3]), "=&v"(tq[4]), "=&v"(tq[5]), "=&v"(tq[6]), "=&v"(tq[7])
          : "v"(vo), "v"(v1), "v"(v2), "v"(v3), "s"(a.wq[0]), "s"(a.wq[1])
          : "memory");
    }
    if (w == 7 && !NO_WQ_PF && !WQ_PF3) {
      const unsigned woff = (unsigned)((16 * j + (lane >> 3)) * (HD * 2) + xcd * 1024 + (lane & 7) * 128);
      asm volatile(
          "s_nop 4\n\t"
          "global_load_dword %0, %4, %6\n\tglobal_load_dword %1, %5, %6\n\t"
          "global_load_dword %2, %4, %7\n\tglobal_load_dword %3, %5, %7"
          : "=&v"(t0), "=&v"(t1), "=&v"(t2), "=&v"(t3)
          : "v"(woff), "v"(woff + 8u * HD * 2), "s"(a.wq[0]), "s"(a.wq[1])
          : "memory");
    }
    const int li = 7 - w;
    {
      const char* xg = a.x[type1 ? 0 : 1] + (size_t)b * (SQ * DM * 2);
      const int nq = type1 ? 4 : 3;
      for (int q = 0; q < nq; ++q) {
        const int rg = li * nq + q;
        const unsigned base = g * 256 + 16 * (r ^ (unsigned)(rg & 3));
        dma4c(xg + rg * 4096, lds0 + xoff + rg * 4096, base, base ^ 64u, base ^ 128u, base ^ 192u);
      }
    }
    const int nu = (type1 && li == 2) ? 2 : 3;
    const char* up[3];
    unsigned ul[3], uo[3][4];
    bool uA[3];
#pragma unroll
    for (int u = 0; u < 3; ++u) {
      const int id = (li * 3 + u) & (type1 ? 7 : 15);
      const int st = id >> 2, isv = (id >> 1) & 1, hf = id & 1;
      uA[u] = type1 && st == 1;
      const size_t sidx = uA[u] ? (size_t)(b * 8 + xcd) : (size_t)(b * 32 + 4 * xcd + (type1 ? 0 : 1 + st));
      const char* kb = uA[u] ? a.k[0] : a.k[1];
      const char* vb = uA[u] ? a.v[0] : a.v[1];
      up[u] = (isv ? vb : kb) + sidx * (SK * DH * 2) + hf * 4096;
      ul[u] = lds0 + st * BLK_BYTES + isv * 8192 + hf * 4096;
#pragma unroll
      for (int q = 0; q < 4; ++q) uo[u][q] = g * 256 + 16 * (r ^ (isv ? (((q & 1) * 4 + g) << 1) : ((g << 2) | q)));
    }
    const unsigned sstride = nst * BLK_BYTES;
    auto issue = [&](int blk, int slot, int which) {
#pragma unroll
      for (int u = 0; u < 3; ++u)
        if (u < nu && (which == 0 || (which == 2) == uA[u])) dma4(up[u] + blk * 8192, ul[u] + slot * sstride, uo[u][0], uo[u][1], uo[u][2], uo[u][3]);
    };
    int slot = rdepth - 1;
    if (type1) {
      issue(0, 0, 0); issue(1, 1, 0); issue(2, 2, 0); issue(3, 3, 1);
      u32x4 rb[NRB][3][4];
#pragma unroll
      for (int u = 0; u < 3; ++u) {
        const rsrc_t rs = mkrs(up[u < nu ? u : 0], SK * DH * 2 - 4096);
#pragma unroll
        for (int k = 0; k < NRB; ++k)
#pragma unroll
          for (int q = 0; q < 4; ++q)
            rb[k][u][q] = (u < nu) ? __builtin_amdgcn_raw_buffer_load_b128(rs, uo[u][q] + q * 1024, (RB0 + k * RBS) * 8192, 2) : u32x4{0, 0, 0, 0};
      }
      asm volatile("s_waitcnt vmcnt(%0)" ::"n"(8 * NRB) : "memory");
      BAR();
      BAR();
      WAIT_VM(0);
#pragma unroll
      for (int k = 0; k < NRB; ++k)
#pragma unroll
        for (int u = 0; u < 3; ++u) asm volatile("" : "+v"(rb[k][u][0]), "+v"(rb[k][u][1]), "+v"(rb[k][u][2]), "+v"(rb[k][u][3]));
      auto isreg = [&](int blk) { return blk >= RB0 && blk < RB0 + NRB * RBS && (blk - RB0) % RBS == 0; };
      for (int i = 0; i < NBLK; ++i) {
        int cnt = 0;
#pragma unroll
        for (int d = 1; d <= 3; ++d) cnt += (i + d < NBLK && !isreg(i + d)) ? 1 : 0;
        if (nu == 3) { if (cnt == 3) WAIT_VM(36); else if (cnt == 2) WAIT_VM(24); else if (cnt == 1) WAIT_VM(12); else WAIT_VM(0); }
        else { if (cnt == 3) WAIT_VM(24); else if (cnt == 2) WAIT_VM(16); else if (cnt == 1) WAIT_VM(8); else WAIT_VM(0); }
        BAR();
        if (i == 0) issue(3, 3, 2);
        if (isreg(i + 4)) {
#pragma unroll
          for (int k = 0; k < NRB; ++k)
            if (i + 4 == RB0 + k * RBS) {
#pragma unroll
              for (int u = 0; u < 3; ++u)
                if (u < nu)
#pragma unroll
                  for (int q = 0; q < 4; ++q)
                    *(LAS u32x4*)(uintptr_t)(ul[u] + slot * sstride + q * 1024 + lane * 16) = rb[k][u][q];
            }
          WAIT_LGKM0();
        } else if (i + 4 < NBLK) {
          issue(i + 4, slot, 0);
        }
        slot = (slot + 1 == rdepth) ? 0 : slot + 1;
      }
    } else {
#pragma unroll
      for (int u = 0; u < 3; ++u) tq[u] = __builtin_amdgcn_raw_buffer_load_b32(mkrs(up[u], 4096), 0, 0, 2);
      WAIT_VM(3);
      BAR();
      BAR();
      issue(0, 0, 0); issue(1, 1, 0);
      for (int i = 0; i < NBLK; ++i) {
        if (i + 1 < NBLK) WAIT_VM(12); else WAIT_VM(0);
        BAR();
        if (i + 2 < NBLK) issue(i + 2, slot, 0);
        slot = (slot + 1 == rdepth) ? 0 : slot + 1;
      }
    }
    asm volatile("" ::"v"(t0), "v"(t1), "v"(t2), "v"(t3));
#pragma unroll
    for (int q = 0; q < 8; ++q) asm volatile("" ::"v"(tq[q]));
    return;
  }
  if (!type1 && w == 3) {
    unsigned acc = 0;
    if (WQ_PF3 == 1) {
      const rsrc_t r0 = mkrs(a.wq[0], DM * HD * 2), r1 = mkrs(a.wq[1], DM * HD * 2);
      const unsigned vo = (unsigned)((32 * b + (lane >> 3)) * (HD * 2) + xcd * 1024 + (lane & 7) * 128);
      unsigned t[8];
#pragma unroll
      for (int q = 0; q < 4; ++q) {
        t[2 * q] = __builtin_amdgcn_raw_buffer_load_b32(r0, vo, q * 8 * (HD * 2), WQPF_AUX);
        t[2 * q + 1] = __builtin_amdgcn_raw_buffer_load_b32(r1, vo, q * 8 * (HD * 2), WQPF_AUX);
      }
      BAR();
#pragma unroll
      for (int q = 0; q < 8; ++q) asm volatile("" ::"v"(t[q]));
    } else {
      BAR();
    }
    BAR();
    for (int i = 0; i < WO_PF_IT; ++i) BAR();
    const rsrc_t rw = mkrs(a.wo[xcd & 1], HD * DM * 2);
    const unsigned vo = (unsigned)((256 * b + (lane >> 2)) * (DM * 2) + (xcd >> 2) * 512 + (lane & 3) * 128);
    unsigned t[16];
#pragma unroll
    for (int q = 0; q < 16; ++q) t[q] = __builtin_amdgcn_raw_buffer_load_b32(rw, vo, q * 16 * (DM * 2), 0);
#pragma unroll
    for (int q = 0; q < 16; ++q) asm volatile("" ::"v"(t[q]));
    (void)acc;
    return;
  }

  const bool roleA = type1 && w < 4;
  const int p = roleA ? 0 : 1;
  const int h = roleA ? 4 * xcd + w : (type1 ? 4 * xcd : 4 * xcd + 1 + w);
  const unsigned ringoff = (unsigned)((type1 ? (roleA ? 1 : 0) : w) * BLK_BYTES);
  const bool xlds = !(type1 && w == 4);

  u32x4 qf[3][4];
  if (xlds) qproj<true>(qf, a.wq[p] + h * 256, DM * HD * 2 - h * 256, a.x[p] + b * (SQ * DM * 2), smem, xoff, lane);
  else qproj<false>(qf, a.wq[p] + h * 256, DM * HD * 2 - h * 256, a.x[p] + b * (SQ * DM * 2), smem, xoff, lane);

  int lane2 = lane;
  asm volatile("" : "+v"(lane2));
  const int g = lane2 >> 4, r = lane2 & 15;
  f32x4 oacc[3][8];
#pragma unroll
  for (int t = 0; t < 3; ++t)
#pragma unroll
    for (int c = 0; c < 8; ++c) oacc[t][c] = f32x4{0.f, 0.f, 0.f, 0.f};
  float mrow[3] = {-__builtin_inff(), -__builtin_inff(), -__builtin_inff()};
  float thr[3] = {-__builtin_inff(), -__builtin_inff(), -__builtin_inff()};
  f32x4 lsum[3] = {f32x4{0.f, 0.f, 0.f, 0.f}, f32x4{0.f, 0.f, 0.f, 0.f}, f32x4{0.f, 0.f, 0.f, 0.f}};
  u32x4 ones = {0x3f803f80u, 0x3f803f80u, 0x3f803f80u, 0x3f803f80u};
  asm volatile("" : "+v"(ones));
  const unsigned fk = ((r & 3) << 2) | (r >> 2);
  unsigned kra[4];
#pragma unroll
  for (int s = 0; s < 4; ++s) kra[s] = r * 256 + 16 * ((4 * g + s) ^ fk);
  const unsigned vrow = 4 * g + (r >> 2);
  const unsigned vra = 8192 + vrow * 256 + 8 * (r & 1);
  const unsigned vsw = (vrow & 7) << 1, vp = (r >> 1) & 1;

  int slot = 0;
  for (int i = 0; i < NBLK; ++i) {
    const unsigned sb = ringoff + slot * nst * BLK_BYTES;
    WAIT_LGKM0();
    BAR();
    f32x4 sc[3][2];
    u32x4 kf[8], vf[8], pf[3];
    float lm = 0.f, pv[8];
#pragma unroll
    for (int s = 0; s < 8; ++s) kf[s] = *(const LAS u32x4*)((LAS char*)smem + sb + kra[s & 3] + (s >> 2) * 4096);
    auto qk = [&](int t, int s) {
      sc[t][s >> 2] = mfma16(kf[s], qf[t][s & 3], (s & 3) ? sc[t][s >> 2] : f32x4{0.f, 0.f, 0.f, 0.f});
    };
    auto ldv = [&](int c) {
      const unsigned ad = sb + vra + 16 * ((2 * c + vp) ^ vsw);
      s16x4 l0 = __builtin_amdgcn_ds_read_tr16_b64_v4i16((LAS s16x4*)((LAS char*)smem + ad));
      s16x4 l1 = __builtin_amdgcn_ds_read_tr16_b64_v4i16((LAS s16x4*)((LAS char*)smem + ad + 4096));
      u32x2 u0 = __builtin_bit_cast(u32x2, l0), u1 = __builtin_bit_cast(u32x2, l1);
      vf[c] = u32x4{u0[0], u0[1], u1[0], u1[1]};
    };
    auto mx = [&](int t, int q) {
      if (q == 0) lm = max3(sc[t][0][0], sc[t][0][1], sc[t][0][2]);
      else if (q == 1) lm = max3(lm, sc[t][0][3], sc[t][1][0]);
      else if (q == 2) lm = max3(lm, sc[t][1][1], sc[t][1][2]);
      else lm = __builtin_elementwise_maximum(lm, sc[t][1][3]);
    };
    auto rescale = [&](int t) {
      if (__builtin_expect(__any(!(lm <= thr[t])), 0)) {
        const float mn = fmaxf(mrow[t], xg_max(lm) * SCL);
        const float al = __builtin_amdgcn_exp2f(mrow[t] - mn);
        mrow[t] = mn;
        thr[t] = (mn + RESCALE_THR) * (1.0f / SCL);
        lsum[t] *= al;
#pragma unroll
        for (int c = 0; c < 8; ++c) oacc[t][c] *= al;
      }
    };
    auto ex = [&](int t, int e) {
      pv[e] = __builtin_amdgcn_exp2f(fmaf(sc[t][e >> 2][e & 3], SCL, -mrow[t]));
      if (e & 1) {
        unsigned u = pack2(pv[e - 1], pv[e]);
        asm volatile("" : "+v"(u));
        pf[t][e >> 1] = u;
      } else {
        asm volatile("" : "+v"(pv[e]));
      }
    };
    auto pvm = [&](int t, int c) {
      if (c < 8) oacc[t][c] = mfma16(vf[c], pf[t], oacc[t][c]);
      else lsum[t] = mfma16(ones, pf[t], lsum[t]);
    };
    SB();
#pragma unroll
    for (int s = 0; s < 8; ++s) qk(0, s);
    SB();
#pragma unroll
    for (int s = 0; s < 8; ++s) {
      qk(1, s);
      if (s >= 2 && s < 6) mx(0, s - 2);
      SB();
    }
    rescale(0);
#pragma unroll
    for (int s = 0; s < 8; ++s) {
      qk(2, s);
      ldv(s);
      if (s & 1) ex(0, s >> 1);
      SB();
    }
#pragma unroll
    for (int s = 0; s < 4; ++s) { ex(0, 4 + s); mx(1, s); SB(); }
    rescale(1);
#pragma unroll
    for (int s = 0; s < 9; ++s) {
      pvm(0, s);
      if (s < 8) ex(1, s);
      if (s >= 5) mx(2, s - 5);
      SB();
    }
    rescale(2);
#pragma unroll
    for (int s = 0; s < 9; ++s) {
      pvm(1, s);
      if (s < 8) ex(2, s);
      SB();
    }
#pragma unroll
    for (int s = 0; s < 9; ++s) pvm(2, s);
    slot = (slot + 1 == rdepth) ? 0 : slot + 1;
  }

  char* op = a.o + ((size_t)(p * 512 + b * 32 + h) * 12) * 1024 + lane2 * 16;
#pragma unroll
  for (int t = 0; t < 3; ++t) {
    const float inv = 1.0f / lsum[t][0];
#pragma unroll
    for (int m = 0; m < 4; ++m) {
      u32x4 f;
      f[0] = pack2(oacc[t][2 * m][0] * inv, oacc[t][2 * m][1] * inv);
      f[1] = pack2(oacc[t][2 * m][2] * inv, oacc[t][2 * m][3] * inv);
      f[2] = pack2(oacc[t][2 * m + 1][0] * inv, oacc[t][2 * m + 1][1] * inv);
      f[3] = pack2(oacc[t][2 * m + 1][2] * inv, oacc[t][2 * m + 1][3] * inv);
      *(u32x4*)(op + (t * 4 + m) * 1024) = f;
    }
  }
}

struct OutArgs {
  const char* wo[2];
  const char* o;
  char* out;
};
#define OP_PHASE 0
constexpr int OPD = 5;
constexpr int LDS_OPROJ = 8 * OPD * 4096;
__global__ __launch_bounds__(512, 2) void oproj_kernel(OutArgs a) {
  extern __shared__ __attribute__((aligned(16))) char smem[];
  const int lane = threadIdx.x & 63;
  const int w = __builtin_amdgcn_readfirstlane(threadIdx.x >> 6);
  const int xcd = blockIdx.x & 7, j = blockIdx.x >> 3;
  const int p = xcd & 1, b = ((xcd >> 1) & 1) * 8 + (j & 7), nb = (xcd >> 2) * 4 + (j >> 3);
  const int g = lane >> 4, r = lane & 15;
  const int ph = OP_PHASE ? (2 * (j & 7) + 4 * (j >> 3)) : 0;
  const char* wg = a.wo[p] + (size_t)w * 512 * (DM * 2) + nb * 128;
  const char* og = a.o + (size_t)(p * 512 + b * 32 + 4 * w) * 12288;
  const unsigned lw = (unsigned)(uintptr_t)(LAS char*)smem + w * (OPD * 4096);
  const unsigned so = (lane >> 3) * (DM * 2) + 16 * ((lane & 7) ^ (g << 1));
  const unsigned so1 = so + 7168, so2 = so + 2 * 7168, so3 = so + 3 * 7168, ov = lane * 16;
  const unsigned trow = 4 * g + (r >> 2);
  const unsigned tra = w * (OPD * 4096) + trow * 128 + 8 * (r & 1), tsw = ((trow >> 1) & 3) << 1, tp = (r >> 1) & 1;
  f32x4 acc[3][4];
#pragma unroll
  for (int t = 0; t < 3; ++t)
#pragma unroll
    for (int e = 0; e < 4; ++e) acc[t][e] = f32x4{0.f, 0.f, 0.f, 0.f};
  u32x4 ob[OPD][3];
  auto load = [&](int s) {
    const int q = (s + ph) & 15;
    dma4c(wg + q * (32 * DM * 2), lw + (s % OPD) * 4096, so, so1, so2, so3);
    const char* os = og + (q >> 2) * 12288 + (q & 3) * 1024 + 4096;
    asm volatile(
        "global_load_dwordx4 %0, %3, %4 offset:-4096\n\tglobal_load_dwordx4 %1, %3, %4\n\tglobal_load_dwordx4 %2, %3, %5"
        : "=&v"(ob[s % OPD][0]), "=&v"(ob[s % OPD][1]), "=&v"(ob[s % OPD][2])
        : "v"(ov), "s"(os), "s"(os + 4096)
        : "memory");
  };
#pragma unroll
  for (int s = 0; s < OPD; ++s) load(s);
#pragma unroll
  for (int s = 0; s < 16; ++s) {
    const int left = (15 - s < OPD - 1) ? 15 - s : OPD - 1;
    asm volatile("s_waitcnt vmcnt(%3)" : "+v"(ob[s % OPD][0]), "+v"(ob[s % OPD][1]), "+v"(ob[s % OPD][2]) : "n"(7 * left) : "memory");
    u32x4 f[4];
#pragma unroll
    for (int e = 0; e < 4; ++e) {
      const unsigned ad = tra + (s % OPD) * 4096 + 16 * ((2 * e + tp) ^ tsw);
      s16x4 l0 = __builtin_amdgcn_ds_read_tr16_b64_v4i16((LAS s16x4*)((LAS char*)smem + ad));
      s16x4 l1 = __builtin_amdgcn_ds_read_tr16_b64_v4i16((LAS s16x4*)((LAS char*)smem + ad + 2048));
      u32x2 u0 = __builtin_bit_cast(u32x2, l0), u1 = __builtin_bit_cast(u32x2, l1);
      f[e] = u32x4{u0[0], u0[1], u1[0], u1[1]};
    }
#pragma unroll
    for (int e = 0; e < 4; ++e)
#pragma unroll
      for (int t = 0; t < 3; ++t) acc[t][e] = mfma16(f[e], ob[s % OPD][t], acc[t][e]);
    if (s + OPD < 16) {
      asm volatile("s_waitcnt lgkmcnt(0)" : "+v"(acc[2][3]) : : "memory");
      load(s + OPD);
    }
  }
  __syncthreads();
  f32x4* red = (f32x4*)smem;
#pragma unroll
  for (int t = 0; t < 3; ++t)
#pragma unroll
    for (int e = 0; e < 4; ++e) red[(w * 12 + t * 4 + e) * 64 + (((lane & 15) << 2) | (lane >> 4))] = acc[t][e];
  __syncthreads();
  if (w < 6) {
    const int t = w >> 1, e = (w & 1) * 2 + (lane & 2 ? 1 : 0), q = lane >> 2, gh = lane & 1;
    f32x4 s0 = f32x4{0.f, 0.f, 0.f, 0.f}, s1 = s0;
#pragma unroll
    for (int ww = 0; ww < 8; ++ww) {
      s0 += red[(ww * 12 + t * 4 + e) * 64 + q * 4 + 2 * gh];
      s1 += red[(ww * 12 + t * 4 + e) * 64 + q * 4 + 2 * gh + 1];
    }
    u32x4 f = {pack2(s0[0], s0[1]), pack2(s0[2], s0[3]), pack2(s1[0], s1[1]), pack2(s1[2], s1[3])};
    *(u32x4*)(a.out + ((size_t)(b * SQ + 16 * t + q) * 1024 + p * 512 + nb * 64 + 16 * e + 8 * gh) * 2) = f;
  }
}

extern "C" void kernel_launch(void* const* d_in, const int* in_sizes, int n_in, void* d_out, int out_size, void* d_ws,
                              size_t ws_size, hipStream_t stream) {
  (void)in_sizes; (void)n_in; (void)out_size; (void)ws_size;
  AttnArgs a;
  a.x[0] = (const char*)d_in[0]; a.k[0] = (const char*)d_in[1]; a.v[0] = (const char*)d_in[2]; a.wq[0] = (const char*)d_in[3];
  a.x[1] = (const char*)d_in[5]; a.k[1] = (const char*)d_in[6]; a.v[1] = (const char*)d_in[7]; a.wq[1] = (const char*)d_in[8];
  a.wo[0] = (const char*)d_in[4]; a.wo[1] = (const char*)d_in[9];
  a.o = (char*)d_ws;
  OutArgs oa;
  oa.wo[0] = (const char*)d_in[4]; oa.wo[1] = (const char*)d_in[9]; oa.o = (const char*)d_ws; oa.out = (char*)d_out;
  (void)hipFuncSetAttribute((const void*)attn_kernel, hipFuncAttributeMaxDynamicSharedMemorySize, LDS_ATTN);
  (void)hipFuncSetAttribute((const void*)oproj_kernel, hipFuncAttributeMaxDynamicSharedMemorySize, LDS_OPROJ);
  hipLaunchKernelGGL(attn_kernel, dim3(256), dim3(512), LDS_ATTN, stream, a);
  hipLaunchKernelGGL(oproj_kernel, dim3(256), dim3(512), LDS_OPROJ, stream, oa);
}
```

```cpp
#include <hip/hip_runtime.h>
#include <hip/hip_bf16.h>
#include <stdint.h>

typedef __attribute__((ext_vector_type(8))) __bf16 bf16x8;
typedef __attribute__((ext_vector_type(2))) __bf16 bf16x2;
typedef __attribute__((ext_vector_type(4))) short s16x4;
typedef __attribute__((ext_vector_type(4))) float f32x4;
typedef __attribute__((ext_vector_type(2))) float f32x2;
typedef __attribute__((ext_vector_type(4))) unsigned u32x4;
typedef __attribute__((ext_vector_type(2))) unsigned u32x2;
#define LAS __attribute__((address_space(3)))

#define KV_NT 1
#define NT_STR " nt"

constexpr int SQ = 48, DM = 512, NH = 32, SK = 1024, DH = 128, HD = NH * DH;
constexpr int NBLK = SK / 32;
constexpr int BLK_BYTES = 16384;
constexpr int LDS_ATTN = 10 * BLK_BYTES;
constexpr int XOFF1 = 7 * BLK_BYTES, XOFF2 = 6 * BLK_BYTES;
#define FILL_STEP 13
#define QSTAMP(n)
#define FILL1_LATE 0
#define NO_WQ_PF 0
#define WO_PF_IT 32
#define WO_GRAN 128
#define WO_PER 64
#define X_NT 0
#define WQ_AUX 0
#define WQPF_AUX 0
#define WS 0
#define WOI 31
#define O_AUX 1
#define NRB 2
#define RB0 8
#define RBS 10
#define KPH 0
#define WO_MATCH 1
#define WQ_PF3 2
constexpr float SCL = 0.088388347648318447f * 1.4426950408889634f;
constexpr float RESCALE_THR = 6.0f;

__device__ __forceinline__ unsigned pack2(float lo, float hi) {
  f32x2 f = {lo, hi};
  return __builtin_bit_cast(unsigned, __builtin_convertvector(f, bf16x2));
}
__device__ __forceinline__ f32x4 mfma16(u32x4 a, u32x4 b, f32x4 c) {
  return __builtin_amdgcn_mfma_f32_16x16x32_bf16(__builtin_bit_cast(bf16x8, a), __builtin_bit_cast(bf16x8, b), c, 0, 0, 0);
}
__device__ __forceinline__ float xg_max(float v) {
  auto r = __builtin_amdgcn_permlane16_swap(__float_as_uint(v), __float_as_uint(v), false, false);
  v = fmaxf(__uint_as_float(r[0]), __uint_as_float(r[1]));
  auto s = __builtin_amdgcn_permlane32_swap(__float_as_uint(v), __float_as_uint(v), false, false);
  return fmaxf(__uint_as_float(s[0]), __uint_as_float(s[1]));
}
__device__ __forceinline__ float max3(float a, float b, float c) {
  return __builtin_elementwise_maximum(__builtin_elementwise_maximum(a, b), c);
}
#define SB() __builtin_amdgcn_sched_barrier(0)

#define DMA4_BODY(POL)                                                        \
  unsigned keep;                                                              \
  asm volatile(                                                               \
      "s_nop 4\n\ts_mov_b32 %0, m0\n\ts_mov_b32 m0, %1\n\ts_nop 0\n\t"      \
      "global_load_lds_dwordx4 %3, %2" POL "\n\t"                             \
      "global_load_lds_dwordx4 %4, %2 offset:1024" POL "\n\t"                 \
      "global_load_lds_dwordx4 %5, %2 offset:2048" POL "\n\t"                 \
      "global_load_lds_dwordx4 %6, %2 offset:3072" POL "\n\t"                 \
      "s_mov_b32 m0, %0"                                                      \
      : "=&s"(keep)                                                           \
      : "s"(m), "s"(g), "v"(o0), "v"(o1), "v"(o2), "v"(o3)                    \
      : "memory")
__device__ __forceinline__ void dma4(const char* g, unsigned m, unsigned o0, unsigned o1, unsigned o2, unsigned o3) { DMA4_BODY(NT_STR); }
__device__ __forceinline__ void dma4c(const char* g, unsigned m, unsigned o0, unsigned o1, unsigned o2, unsigned o3) {
  DMA4_BODY("");
}
#define WAIT_VM(n) asm volatile("s_waitcnt vmcnt(" #n ")" ::: "memory")
#define WAIT_LGKM0() asm volatile("s_waitcnt lgkmcnt(0)" ::: "memory")
#define BAR() asm volatile("s_barrier" ::: "memory")

typedef __amdgpu_buffer_rsrc_t rsrc_t;
__device__ __forceinline__ rsrc_t mkrs(const void* p, int bytes) { return __builtin_amdgcn_make_buffer_rsrc((void*)p, (short)0, bytes, 0x00020000); }
__device__ __forceinline__ u32x4 bld128(rsrc_t rs, unsigned vo, unsigned so) { return __builtin_amdgcn_raw_buffer_load_b128(rs, vo, so, 0); }
__device__ __forceinline__ u32x2 bld64(rsrc_t rs, unsigned vo, unsigned so) { return __builtin_amdgcn_raw_buffer_load_b64(rs, vo, so, 0); }

struct AttnArgs {
  const char* x[2];
  const char* k[2];
  const char* v[2];
  const char* wq[2];
  const char* wo[2];
  char* o;
};

template <bool XLDS>
__device__ __forceinline__ void qproj(u32x4 (&qf)[3][4], const char* wq, int wbytes, const char* x, char* smem, unsigned xoff, int lane) {
  f32x4 acc[3][8];
#pragma unroll
  for (int t = 0; t < 3; ++t)
#pragma unroll
    for (int e = 0; e < 8; ++e) acc[t][e] = f32x4{0.f, 0.f, 0.f, 0.f};
  const rsrc_t rw = mkrs(wq, wbytes);
  const rsrc_t rx = mkrs(x, SQ * DM * 2);
  const unsigned g = lane >> 4, r = lane & 15;
  const unsigned wv = g * (32 * HD * 2) + r * 16;
  const unsigned xv = r * (DM * 2) + g * 64;
  const unsigned xl = xoff + r * (DM * 2) + 16 * ((4 * g) ^ (((r & 3) << 2) | (r >> 2)));
  constexpr int XA = XLDS ? 1 : 2;
  u32x4 wb[3][8], xb[XA + 1][3];
  auto loadw = [&](int s, int jj) { wb[s % 3][jj] = bld128(rw, wv, (128 * (s >> 2) + 8 * (s & 3) + jj) * (HD * 2)); };
  auto loadx = [&](int s, int t) {
    if (XLDS) xb[s % (XA + 1)][t] = *(const LAS u32x4*)((LAS char*)smem + (xl ^ (16 * (s & 3))) + t * 16 * DM * 2 + (s >> 2) * 256);
    else xb[s % (XA + 1)][t] = bld128(rx, xv + (s >> 2) * 256 + (s & 3) * 16, t * 16 * DM * 2);
  };
  u32x4 f[2];
  auto tr = [&](int n, int t) {
    const int s = n >> 3, e = n & 7;
    f[n & 1][t] = __builtin_amdgcn_perm(wb[s % 3][2 * t + 1][e >> 1], wb[s % 3][2 * t][e >> 1], (e & 1) ? 0x07060302u : 0x05040100u);
  };
#pragma unroll
  for (int s = 0; s < 2; ++s) {
#pragma unroll
    for (int jj = 0; jj < 8; ++jj) loadw(s, jj);
    if (!XLDS)
#pragma unroll
      for (int t = 0; t < 3; ++t) loadx(s, t);
  }
  BAR();
  QSTAMP(0);
  if (XLDS)
#pragma unroll
    for (int t = 0; t < 3; ++t) loadx(0, t);
#pragma unroll
  for (int t = 0; t < 4; ++t) tr(0, t);
#pragma unroll
  for (int s = 0; s < 16; ++s)
#pragma unroll
  for (int e = 0; e < 8; ++e) {
    const int n = 8 * s + e;
    if (n == 8 * FILL_STEP + 4) BAR();
    SB();
    acc[0][e] = mfma16(f[n & 1], xb[s % (XA + 1)][0], acc[0][e]);
    if (n + 1 < 128) { tr(n + 1, 0); tr(n + 1, 1); }
    SB();
    acc[1][e] = mfma16(f[n & 1], xb[s % (XA + 1)][1], acc[1][e]);
    if (n + 1 < 128) { tr(n + 1, 2); tr(n + 1, 3); }
    SB();
    acc[2][e] = mfma16(f[n & 1], xb[s % (XA + 1)][2], acc[2][e]);
    if (e < 4 && s + 2 < 16) { loadw(s + 2, 2 * e); loadw(s + 2, 2 * e + 1); }
    if (e >= 4 && e < 7 && s + XA < 16) loadx(s + XA, e - 4);
    if (n == 7) QSTAMP(1);
    if (n == 63) QSTAMP(2);
  }
  SB();
#pragma unroll
  for (int t = 0; t < 3; ++t)
#pragma unroll
    for (int s = 0; s < 4; ++s)
#pragma unroll
      for (int e = 0; e < 4; ++e) qf[t][s][e] = pack2(acc[t][2 * e][s], acc[t][2 * e + 1][s]);
}

__global__ __launch_bounds__(512, 2) void attn_kernel(AttnArgs a) {
  extern __shared__ __attribute__((aligned(16))) char smem[];
  const int lane = threadIdx.x & 63;
  const int w = __builtin_amdgcn_readfirstlane(threadIdx.x >> 6);
  const int xcd = blockIdx.x & 7, j = blockIdx.x >> 3;
  const bool type1 = j < 16;
  const int b = j & 15;
  const unsigned lds0 = (unsigned)(uintptr_t)(LAS char*)smem;
  const int nst = type1 ? 2 : 3, rdepth = type1 ? 5 : 3;
  const unsigned xoff = type1 ? XOFF1 : XOFF2;

  auto warm_wo = [&](int nbar, int q0, int q1) {
    const rsrc_t rw = mkrs(a.wo[xcd & 1], HD * DM * 2);
    const unsigned vo = (unsigned)((256 * b + (lane >> 2)) * (DM * 2) + (xcd >> 2) * 512 + (lane & 3) * 128);
    unsigned t[16];
#pragma unroll
    for (int q = 0; q < 16; ++q) t[q] = (q >= q0 && q < q1) ? __builtin_amdgcn_raw_buffer_load_b32(rw, vo, q * 16 * (DM * 2), 0) : 0u;
    for (int i = 0; i < nbar; ++i) BAR();
#pragma unroll
    for (int q = 0; q < 16; ++q) asm volatile("" ::"v"(t[q]));
  };
  if (w >= (type1 ? 5 : 4)) {
    const unsigned g = lane >> 4, r = lane & 15;
    unsigned tq[8];
#pragma unroll
    for (int q = 0; q < 8; ++q) tq[q] = 0;
    if (type1 && w == 5) {
      const unsigned vo = (unsigned)((32 * b + (lane >> 3)) * (HD * 2) + xcd * 1024 + (lane & 7) * 128);
      const unsigned v1 = vo + 8u * HD * 2, v2 = vo + 16u * HD * 2, v3 = vo + 24u * HD * 2;
      asm volatile(
          "s_nop 4\n\t"
          "global_load_dword %0, %8, %13\n\tglobal_load_dword %1, %9, %13\n\tglobal_load_dword %2, %10, %13\n\tglobal_load_dword %3, %11, %13\n\t"
          "global_load_dword %4, %8, %12\n\tglobal_load_dword %5, %9, %12\n\tglobal_load_dword %6, %10, %12\n\tglobal_load_dword %7, %11, %12"
          : "=&v"(tq[0]), "=&v"(tq[1]), "=&v"(tq[2]), "=&v"(tq[3]), "=&v"(tq[4]), "=&v"(tq[5]), "=&v"(tq[6]), "=&v"(tq[7])
          : "v"(vo), "v"(v1), "v"(v2), "v"(v3), "s"(a.wq[0]), "s"(a.wq[1])
          : "memory");
    }
    const int li = 7 - w;
    {
      const char* xg = a.x[type1 ? 0 : 1] + (size_t)b * (SQ * DM * 2);
      const int nq = type1 ? 4 : 3;
      for (int q = 0; q < nq; ++q) {
        const int rg = li * nq + q;
        const unsigned base = g * 256 + 16 * (r ^ (unsigned)(rg & 3));
        dma4c(xg + rg * 4096, lds0 + xoff + rg * 4096, base, base ^ 64u, base ^ 128u, base ^ 192u);
      }
    }
    const int nu = (type1 && li == 2) ? 2 : 3;
    const char* up[3];
    unsigned ul[3], uo[3][4];
    bool uA[3];
#pragma unroll
    for (int u = 0; u < 3; ++u) {
      const int id = (li * 3 + u) & (type1 ? 7 : 15);
      const int st = id >> 2, isv = (id >> 1) & 1, hf = id & 1;
      uA[u] = type1 && st == 1;
      const size_t sidx = uA[u] ? (size_t)(b * 8 + xcd) : (size_t)(b * 32 + 4 * xcd + (type1 ? 0 : 1 + st));
      const char* kb = uA[u] ? a.k[0] : a.k[1];
      const char* vb = uA[u] ? a.v[0] : a.v[1];
      up[u] = (isv ? vb : kb) + sidx * (SK * DH * 2) + hf * 4096;
      ul[u] = lds0 + st * BLK_BYTES + isv * 8192 + hf * 4096;
#pragma unroll
      for (int q = 0; q < 4; ++q) uo[u][q] = g * 256 + 16 * (r ^ (isv ? (((q & 1) * 4 + g) << 1) : ((g << 2) | q)));
    }
    const unsigned sstride = nst * BLK_BYTES;
    auto issue = [&](int blk, int slot, int which) {
#pragma unroll
      for (int u = 0; u < 3; ++u)
        if (u < nu && (which == 0 || (which == 2) == uA[u])) dma4(up[u] + blk * 8192, ul[u] + slot * sstride, uo[u][0], uo[u][1], uo[u][2], uo[u][3]);
    };
    int slot = rdepth - 1;
    if (type1) {
      issue(0, 0, 0); issue(1, 1, 0); issue(2, 2, 0); issue(3, 3, 1);
      u32x4 rb[NRB][3][4];
#pragma unroll
      for (int u = 0; u < 3; ++u) {
        const rsrc_t rs = mkrs(up[u < nu ? u : 0], SK * DH * 2 - 4096);
#pragma unroll
        for (int k = 0; k < NRB; ++k)
#pragma unroll
          for (int q = 0; q < 4; ++q)
            rb[k][u][q] = (u < nu) ? __builtin_amdgcn_raw_buffer_load_b128(rs, uo[u][q] + q * 1024, (RB0 + k * RBS) * 8192, 2) : u32x4{0, 0, 0, 0};
      }
      asm volatile("s_waitcnt vmcnt(%0)" ::"n"(8 * NRB) : "memory");
      BAR();
      BAR();
      WAIT_VM(0);
#pragma unroll
      for (int k = 0; k < NRB; ++k)
#pragma unroll
        for (int u = 0; u < 3; ++u) asm volatile("" : "+v"(rb[k][u][0]), "+v"(rb[k][u][1]), "+v"(rb[k][u][2]), "+v"(rb[k][u][3]));
      auto isreg = [&](int blk) { return blk >= RB0 && blk < RB0 + NRB * RBS && (blk - RB0) % RBS == 0; };
      for (int i = 0; i < NBLK; ++i) {
        int cnt = 0;
#pragma unroll
        for (int d = 1; d <= 3; ++d) cnt += (i + d < NBLK && !isreg(i + d)) ? 1 : 0;
        if (nu == 3) { if (cnt == 3) WAIT_VM(36); else if (cnt == 2) WAIT_VM(24); else if (cnt == 1) WAIT_VM(12); else WAIT_VM(0); }
        else { if (cnt == 3) WAIT_VM(24); else if (cnt == 2) WAIT_VM(16); else if (cnt == 1) WAIT_VM(8); else WAIT_VM(0); }
        BAR();
        if (i == 0) issue(3, 3, 2);
        if (isreg(i + 4)) {
#pragma unroll
          for (int k = 0; k < NRB; ++k)
            if (i + 4 == RB0 + k * RBS) {
#pragma unroll
              for (int u = 0; u < 3; ++u)
                if (u < nu)
#pragma unroll
                  for (int q = 0; q < 4; ++q)
                    *(LAS u32x4*)(uintptr_t)(ul[u] + slot * sstride + q * 1024 + lane * 16) = rb[k][u][q];
            }
          WAIT_LGKM0();
        } else if (i + 4 < NBLK) {
          issue(i + 4, slot, 0);
        }
        slot = (slot + 1 == rdepth) ? 0 : slot + 1;
        if (WS > 0 && i == WOI && li == 2) {
          WAIT_VM(0);
          warm_wo(NBLK - 1 - WOI, 0, WS);
          break;
        }
      }
    } else {
      WAIT_VM(0);
      BAR();
      BAR();
      issue(0, 0, 0); issue(1, 1, 0);
      for (int i = 0; i < NBLK; ++i) {
        if (i + 1 < NBLK) WAIT_VM(12); else WAIT_VM(0);
        BAR();
        if (i + 2 < NBLK) issue(i + 2, slot, 0);
        slot = (slot + 1 == rdepth) ? 0 : slot + 1;
      }
    }
#pragma unroll
    for (int q = 0; q < 8; ++q) asm volatile("" ::"v"(tq[q]));
    return;
  }
  if (!type1 && w == 3) {
    if (WS == 16) return;
    for (int i = 0; i < WO_PF_IT + 2; ++i) BAR();
    warm_wo(NBLK - WO_PF_IT, WS, 16);
    return;
  }

  const bool roleA = type1 && w < 4;
  const int p = roleA ? 0 : 1;
  const int h = roleA ? 4 * xcd + w : (type1 ? 4 * xcd : 4 * xcd + 1 + w);
  const unsigned ringoff = (unsigned)((type1 ? (roleA ? 1 : 0) : w) * BLK_BYTES);
  const bool xlds = !(type1 && w == 4);

  u32x4 qf[3][4];
  if (xlds) qproj<true>(qf, a.wq[p] + h * 256, DM * HD * 2 - h * 256, a.x[p] + b * (SQ * DM * 2), smem, xoff, lane);
  else qproj<false>(qf, a.wq[p] + h * 256, DM * HD * 2 - h * 256, a.x[p] + b * (SQ * DM * 2), smem, xoff, lane);

  int lane2 = lane;
  asm volatile("" : "+v"(lane2));
  const int g = lane2 >> 4, r = lane2 & 15;
  f32x4 oacc[3][8];
#pragma unroll
  for (int t = 0; t < 3; ++t)
#pragma unroll
    for (int c = 0; c < 8; ++c) oacc[t][c] = f32x4{0.f, 0.f, 0.f, 0.f};
  float mrow[3] = {-__builtin_inff(), -__builtin_inff(), -__builtin_inff()};
  float thr[3] = {-__builtin_inff(), -__builtin_inff(), -__builtin_inff()};
  f32x4 lsum[3] = {f32x4{0.f, 0.f, 0.f, 0.f}, f32x4{0.f, 0.f, 0.f, 0.f}, f32x4{0.f, 0.f, 0.f, 0.f}};
  u32x4 ones = {0x3f803f80u, 0x3f803f80u, 0x3f803f80u, 0x3f803f80u};
  asm volatile("" : "+v"(ones));
  const unsigned fk = ((r & 3) << 2) | (r >> 2);
  unsigned kra[4];
#pragma unroll
  for (int s = 0; s < 4; ++s) kra[s] = r * 256 + 16 * ((4 * g + s) ^ fk);
  const unsigned vrow = 4 * g + (r >> 2);
  const unsigned vra = 8192 + vrow * 256 + 8 * (r & 1);
  const unsigned vsw = (vrow & 7) << 1, vp = (r >> 1) & 1;

  int slot = 0;
  for (int i = 0; i < NBLK; ++i) {
    const unsigned sb = ringoff + slot * nst * BLK_BYTES;
    WAIT_LGKM0();
    BAR();
    f32x4 sc[3][2];
    u32x4 kf[8], vf[8], pf[3];
    float lm = 0.f, pv[8];
#pragma unroll
    for (int s = 0; s < 8; ++s) kf[s] = *(const LAS u32x4*)((LAS char*)smem + sb + kra[s & 3] + (s >> 2) * 4096);
    auto qk = [&](int t, int s) {
      sc[t][s >> 2] = mfma16(kf[s], qf[t][s & 3], (s & 3) ? sc[t][s >> 2] : f32x4{0.f, 0.f, 0.f, 0.f});
    };
    auto ldv = [&](int c) {
      const unsigned ad = sb + vra + 16 * ((2 * c + vp) ^ vsw);
      s16x4 l0 = __builtin_amdgcn_ds_read_tr16_b64_v4i16((LAS s16x4*)((LAS char*)smem + ad));
      s16x4 l1 = __builtin_amdgcn_ds_read_tr16_b64_v4i16((LAS s16x4*)((LAS char*)smem + ad + 4096));
      u32x2 u0 = __builtin_bit_cast(u32x2, l0), u1 = __builtin_bit_cast(u32x2, l1);
      vf[c] = u32x4{u0[0], u0[1], u1[0], u1[1]};
    };
    auto mx = [&](int t, int q) {
      if (q == 0) lm = max3(sc[t][0][0], sc[t][0][1], sc[t][0][2]);
      else if (q == 1) lm = max3(lm, sc[t][0][3], sc[t][1][0]);
      else if (q == 2) lm = max3(lm, sc[t][1][1], sc[t][1][2]);
      else lm = __builtin_elementwise_maximum(lm, sc[t][1][3]);
    };
    auto rescale = [&](int t) {
      if (__builtin_expect(__any(!(lm <= thr[t])), 0)) {
        const float mn = fmaxf(mrow[t], xg_max(lm) * SCL);
        const float al = __builtin_amdgcn_exp2f(mrow[t] - mn);
        mrow[t] = mn;
        thr[t] = (mn + RESCALE_THR) * (1.0f / SCL);
        lsum[t] *= al;
#pragma unroll
        for (int c = 0; c < 8; ++c) oacc[t][c] *= al;
      }
    };
    auto ex = [&](int t, int e) {
      pv[e] = __builtin_amdgcn_exp2f(fmaf(sc[t][e >> 2][e & 3], SCL, -mrow[t]));
      if (e & 1) {
        unsigned u = pack2(pv[e - 1], pv[e]);
        asm volatile("" : "+v"(u));
        pf[t][e >> 1] = u;
      } else {
        asm volatile("" : "+v"(pv[e]));
      }
    };
    auto pvm = [&](int t, int c) {
      if (c < 8) oacc[t][c] = mfma16(vf[c], pf[t], oacc[t][c]);
      else lsum[t] = mfma16(ones, pf[t], lsum[t]);
    };
    SB();
#pragma unroll
    for (int s = 0; s < 8; ++s) qk(0, s);
    SB();
#pragma unroll
    for (int s = 0; s < 8; ++s) {
      qk(1, s);
      if (s >= 2 && s < 6) mx(0, s - 2);
      SB();
    }
    rescale(0);
#pragma unroll
    for (int s = 0; s < 8; ++s) {
      qk(2, s);
      ldv(s);
      if (s & 1) ex(0, s >> 1);
      SB();
    }
#pragma unroll
    for (int s = 0; s < 4; ++s) { ex(0, 4 + s); mx(1, s); SB(); }
    rescale(1);
#pragma unroll
    for (int s = 0; s < 9; ++s) {
      pvm(0, s);
      if (s < 8) ex(1, s);
      if (s >= 5) mx(2, s - 5);
      SB();
    }
    rescale(2);
#pragma unroll
    for (int s = 0; s < 9; ++s) {
      pvm(1, s);
      if (s < 8) ex(2, s);
      SB();
    }
#pragma unroll
    for (int s = 0; s < 9; ++s) pvm(2, s);
    slot = (slot + 1 == rdepth) ? 0 : slot + 1;
  }

  char* op = a.o + ((size_t)(p * 512 + b * 32 + h) * 12) * 1024 + lane2 * 16;
#pragma unroll
  for (int t = 0; t < 3; ++t) {
    const float inv = 1.0f / lsum[t][0];
#pragma unroll
    for (int m = 0; m < 4; ++m) {
      u32x4 f;
      f[0] = pack2(oacc[t][2 * m][0] * inv, oacc[t][2 * m][1] * inv);
      f[1] = pack2(oacc[t][2 * m][2] * inv, oacc[t][2 * m][3] * inv);
      f[2] = pack2(oacc[t][2 * m + 1][0] * inv, oacc[t][2 * m + 1][1] * inv);
      f[3] = pack2(oacc[t][2 * m + 1][2] * inv, oacc[t][2 * m + 1][3] * inv);
      asm volatile("global_store_dwordx4 %0, %1, off sc0 sc1\n\ts_nop 2" ::"v"(op + (t * 4 + m) * 1024), "v"(f) : "memory");
    }
  }
}

struct OutArgs {
  const char* wo[2];
  const char* o;
  char* out;
};
#define OP_PHASE 0
constexpr int OPD = 5;
constexpr int LDS_OPROJ = 8 * OPD * 4096;
__global__ __launch_bounds__(512, 2) void oproj_kernel(OutArgs a) {
  extern __shared__ __attribute__((aligned(16))) char smem[];
  const int lane = threadIdx.x & 63;
  const int w = __builtin_amdgcn_readfirstlane(threadIdx.x >> 6);
  const int xcd = blockIdx.x & 7, j = blockIdx.x >> 3;
  const int p = xcd & 1, b = ((xcd >> 1) & 1) * 8 + (j & 7), nb = (xcd >> 2) * 4 + (j >> 3);
  const int g = lane >> 4, r = lane & 15;
  const int ph = OP_PHASE == 1 ? (2 * (j & 7) + 4 * (j >> 3)) : OP_PHASE == 2 ? 4 * (j >> 3) : OP_PHASE == 3 ? 8 * (j >> 4) : 0;
  const char* wg = a.wo[p] + (size_t)w * 512 * (DM * 2) + nb * 128;
  const char* og = a.o + (size_t)(p * 512 + b * 32 + 4 * w) * 12288;
  const unsigned lw = (unsigned)(uintptr_t)(LAS char*)smem + w * (OPD * 4096);
  const unsigned so = (lane >> 3) * (DM * 2) + 16 * ((lane & 7) ^ (g << 1));
  const unsigned so1 = so + 7168, so2 = so + 2 * 7168, so3 = so + 3 * 7168, ov = lane * 16;
  const unsigned trow = 4 * g + (r >> 2);
  const unsigned tra = w * (OPD * 4096) + trow * 128 + 8 * (r & 1), tsw = ((trow >> 1) & 3) << 1, tp = (r >> 1) & 1;
  f32x4 acc[3][4];
#pragma unroll
  for (int t = 0; t < 3; ++t)
#pragma unroll
    for (int e = 0; e < 4; ++e) acc[t][e] = f32x4{0.f, 0.f, 0.f, 0.f};
  u32x4 ob[OPD][3];
  auto load = [&](int s) {
    const int q = (s + ph) & 15;
    dma4c(wg + q * (32 * DM * 2), lw + (s % OPD) * 4096, so, so1, so2, so3);
    const char* os = og + (q >> 2) * 12288 + (q & 3) * 1024 + 4096;
    asm volatile(
        "global_load_dwordx4 %0, %3, %4 offset:-4096\n\tglobal_load_dwordx4 %1, %3, %4\n\tglobal_load_dwordx4 %2, %3, %5"
        : "=&v"(ob[s % OPD][0]), "=&v"(ob[s % OPD][1]), "=&v"(ob[s % OPD][2])
        : "v"(ov), "s"(os), "s"(os + 4096)
        : "memory");
  };
#pragma unroll
  for (int s = 0; s < OPD; ++s) load(s);
#pragma unroll
  for (int s = 0; s < 16; ++s) {
    const int left = (15 - s < OPD - 1) ? 15 - s : OPD - 1;
    asm volatile("s_waitcnt vmcnt(%3)" : "+v"(ob[s % OPD][0]), "+v"(ob[s % OPD][1]), "+v"(ob[s % OPD][2]) : "n"(7 * left) : "memory");
    u32x4 f[4];
#pragma unroll
    for (int e = 0; e < 4; ++e) {
      const unsigned ad = tra + (s % OPD) * 4096 + 16 * ((2 * e + tp) ^ tsw);
      s16x4 l0 = __builtin_amdgcn_ds_read_tr16_b64_v4i16((LAS s16x4*)((LAS char*)smem + ad));
      s16x4 l1 = __builtin_amdgcn_ds_read_tr16_b64_v4i16((LAS s16x4*)((LAS char*)smem + ad + 2048));
      u32x2 u0 = __builtin_bit_cast(u32x2, l0), u1 = __builtin_bit_cast(u32x2, l1);
      f[e] = u32x4{u0[0], u0[1], u1[0], u1[1]};
    }
#pragma unroll
    for (int e = 0; e < 4; ++e)
#pragma unroll
      for (int t = 0; t < 3; ++t) acc[t][e] = mfma16(f[e], ob[s % OPD][t], acc[t][e]);
    if (s + OPD < 16) {
      asm volatile("s_waitcnt lgkmcnt(0)" : "+v"(acc[2][3]) : : "memory");
      load(s + OPD);
    }
  }
  f32x4* red = (f32x4*)smem;
  constexpr int RS = OPD * 4096 / 16;
#pragma unroll
  for (int t = 0; t < 3; ++t)
#pragma unroll
    for (int e = 0; e < 4; ++e) red[w * RS + (t * 4 + e) * 64 + (((lane & 15) << 2) | (lane >> 4))] = acc[t][e];
  __syncthreads();
  if (w < 6) {
    const int t = w >> 1, e = (w & 1) * 2 + (lane & 2 ? 1 : 0), q = lane >> 2, gh = lane & 1;
    f32x4 s0 = f32x4{0.f, 0.f, 0.f, 0.f}, s1 = s0;
#pragma unroll
    for (int ww = 0; ww < 8; ++ww) {
      s0 += red[ww * RS + (t * 4 + e) * 64 + q * 4 + 2 * gh];
      s1 += red[ww * RS + (t * 4 + e) * 64 + q * 4 + 2 * gh + 1];
    }
    u32x4 f = {pack2(s0[0], s0[1]), pack2(s0[2], s0[3]), pack2(s1[0], s1[1]), pack2(s1[2], s1[3])};
    *(u32x4*)(a.out + ((size_t)(b * SQ + 16 * t + q) * 1024 + p * 512 + nb * 64 + 16 * e + 8 * gh) * 2) = f;
  }
}

extern "C" void kernel_launch(void* const* d_in, const int* in_sizes, int n_in, void* d_out, int out_size, void* d_ws,
                              size_t ws_size, hipStream_t stream) {
  (void)in_sizes; (void)n_in; (void)out_size; (void)ws_size;
  AttnArgs a;
  a.x[0] = (const char*)d_in[0]; a.k[0] = (const char*)d_in[1]; a.v[0] = (const char*)d_in[2]; a.wq[0] = (const char*)d_in[3];
  a.x[1] = (const char*)d_in[5]; a.k[1] = (const char*)d_in[6]; a.v[1] = (const char*)d_in[7]; a.wq[1] = (const char*)d_in[8];
  a.wo[0] = (const char*)d_in[4]; a.wo[1] = (const char*)d_in[9];
  a.o = (char*)d_ws;
  OutArgs oa;
  oa.wo[0] = (const char*)d_in[4]; oa.wo[1] = (const char*)d_in[9]; oa.o = (const char*)d_ws; oa.out = (char*)d_out;
  (void)hipFuncSetAttribute((const void*)attn_kernel, hipFuncAttributeMaxDynamicSharedMemorySize, LDS_ATTN);
  (void)hipFuncSetAttribute((const void*)oproj_kernel, hipFuncAttributeMaxDynamicSharedMemorySize, LDS_OPROJ);
  hipLaunchKernelGGL(attn_kernel, dim3(256), dim3(512), LDS_ATTN, stream, a);
  hipLaunchKernelGGL(oproj_kernel, dim3(256), dim3(512), LDS_OPROJ, stream, oa);
}
```

```cpp
#include <hip/hip_runtime.h>
#include <hip/hip_bf16.h>
#include <stdint.h>

typedef __attribute__((ext_vector_type(8))) __bf16 bf16x8;
typedef __attribute__((ext_vector_type(2))) __bf16 bf16x2;
typedef __attribute__((ext_vector_type(4))) short s16x4;
typedef __attribute__((ext_vector_type(4))) float f32x4;
typedef __attribute__((ext_vector_type(2))) float f32x2;
typedef __attribute__((ext_vector_type(4))) unsigned u32x4;
typedef __attribute__((ext_vector_type(2))) unsigned u32x2;
#define LAS __attribute__((address_space(3)))

#define KV_NT 1
#define NT_STR " nt"

constexpr int SQ = 48, DM = 512, NH = 32, SK = 1024, DH = 128, HD = NH * DH;
constexpr int NBLK = SK / 32;
constexpr int BLK_BYTES = 16384;
constexpr int LDS_ATTN = 10 * BLK_BYTES;
constexpr int QOFF = 6 * BLK_BYTES;
constexpr int XOFF1 = 7 * BLK_BYTES, XOFF2 = 6 * BLK_BYTES;
#define FILL_STEP 13
#define QSTAMP(n)
#define FILL1_LATE 0
#define NO_WQ_PF 0
#define WO_PF_IT 32
#define WO_GRAN 128
#define WO_PER 64
#define X_NT 0
#define WQ_AUX 0
#define WQPF_AUX 0
#define WS 0
#define WOI 31
#define O_AUX 1
#define NRB 2
#define RB0 8
#define RBS 10
#define KPH 0
#define WO_MATCH 1
#define WQ_PF3 2
constexpr float SCL = 0.088388347648318447f * 1.4426950408889634f;
constexpr float RESCALE_THR = 6.0f;

__device__ __forceinline__ unsigned pack2(float lo, float hi) {
  f32x2 f = {lo, hi};
  return __builtin_bit_cast(unsigned, __builtin_convertvector(f, bf16x2));
}
__device__ __forceinline__ f32x4 mfma16(u32x4 a, u32x4 b, f32x4 c) {
  return __builtin_amdgcn_mfma_f32_16x16x32_bf16(__builtin_bit_cast(bf16x8, a), __builtin_bit_cast(bf16x8, b), c, 0, 0, 0);
}
__device__ __forceinline__ float xg_max(float v) {
  auto r = __builtin_amdgcn_permlane16_swap(__float_as_uint(v), __float_as_uint(v), false, false);
  v = fmaxf(__uint_as_float(r[0]), __uint_as_float(r[1]));
  auto s = __builtin_amdgcn_permlane32_swap(__float_as_uint(v), __float_as_uint(v), false, false);
  return fmaxf(__uint_as_float(s[0]), __uint_as_float(s[1]));
}
__device__ __forceinline__ float max3(float a, float b, float c) {
  return __builtin_elementwise_maximum(__builtin_elementwise_maximum(a, b), c);
}
#define SB() __builtin_amdgcn_sched_barrier(0)

#define DMA4_BODY(POL)                                                        \
  unsigned keep;                                                              \
  asm volatile(                                                               \
      "s_nop 4\n\ts_mov_b32 %0, m0\n\ts_mov_b32 m0, %1\n\ts_nop 0\n\t"      \
      "global_load_lds_dwordx4 %3, %2" POL "\n\t"                             \
      "global_load_lds_dwordx4 %4, %2 offset:1024" POL "\n\t"                 \
      "global_load_lds_dwordx4 %5, %2 offset:2048" POL "\n\t"                 \
      "global_load_lds_dwordx4 %6, %2 offset:3072" POL "\n\t"                 \
      "s_mov_b32 m0, %0"                                                      \
      : "=&s"(keep)                                                           \
      : "s"(m), "s"(g), "v"(o0), "v"(o1), "v"(o2), "v"(o3)                    \
      : "memory")
__device__ __forceinline__ void dma4(const char* g, unsigned m, unsigned o0, unsigned o1, unsigned o2, unsigned o3) { DMA4_BODY(NT_STR); }
__device__ __forceinline__ void dma4c(const char* g, unsigned m, unsigned o0, unsigned o1, unsigned o2, unsigned o3) {
  DMA4_BODY("");
}
#define WAIT_VM(n) asm volatile("s_waitcnt vmcnt(" #n ")" ::: "memory")
#define WAIT_LGKM0() asm volatile("s_waitcnt lgkmcnt(0)" ::: "memory")
#define BAR() asm volatile("s_barrier" ::: "memory")

typedef __amdgpu_buffer_rsrc_t rsrc_t;
__device__ __forceinline__ rsrc_t mkrs(const void* p, int bytes) { return __builtin_amdgcn_make_buffer_rsrc((void*)p, (short)0, bytes, 0x00020000); }
__device__ __forceinline__ u32x4 bld128(rsrc_t rs, unsigned vo, unsigned so) { return __builtin_amdgcn_raw_buffer_load_b128(rs, vo, so, 0); }
__device__ __forceinline__ u32x2 bld64(rsrc_t rs, unsigned vo, unsigned so) { return __builtin_amdgcn_raw_buffer_load_b64(rs, vo, so, 0); }

struct AttnArgs {
  const char* x[2];
  const char* k[2];
  const char* v[2];
  const char* wq[2];
  const char* wo[2];
  char* o;
};

template <bool XLDS>
__device__ __forceinline__ void qproj(u32x4 (&qf)[3][4], const char* wq, int wbytes, const char* x, char* smem, unsigned xoff, int lane) {
  f32x4 acc[3][8];
#pragma unroll
  for (int t = 0; t < 3; ++t)
#pragma unroll
    for (int e = 0; e < 8; ++e) acc[t][e] = f32x4{0.f, 0.f, 0.f, 0.f};
  const rsrc_t rw = mkrs(wq, wbytes);
  const rsrc_t rx = mkrs(x, SQ * DM * 2);
  const unsigned g = lane >> 4, r = lane & 15;
  const unsigned wv = g * (32 * HD * 2) + r * 16;
  const unsigned xv = r * (DM * 2) + g * 64;
  const unsigned xl = xoff + r * (DM * 2) + 16 * ((4 * g) ^ (((r & 3) << 2) | (r >> 2)));
  constexpr int XA = XLDS ? 1 : 2;
  u32x4 wb[3][8], xb[XA + 1][3];
  auto loadw = [&](int s, int jj) { wb[s % 3][jj] = bld128(rw, wv, (128 * (s >> 2) + 8 * (s & 3) + jj) * (HD * 2)); };
  auto loadx = [&](int s, int t) {
    if (XLDS) xb[s % (XA + 1)][t] = *(const LAS u32x4*)((LAS char*)smem + (xl ^ (16 * (s & 3))) + t * 16 * DM * 2 + (s >> 2) * 256);
    else xb[s % (XA + 1)][t] = bld128(rx, xv + (s >> 2) * 256 + (s & 3) * 16, t * 16 * DM * 2);
  };
  u32x4 f[2];
  auto tr = [&](int n, int t) {
    const int s = n >> 3, e = n & 7;
    f[n & 1][t] = __builtin_amdgcn_perm(wb[s % 3][2 * t + 1][e >> 1], wb[s % 3][2 * t][e >> 1], (e & 1) ? 0x07060302u : 0x05040100u);
  };
#pragma unroll
  for (int s = 0; s < 2; ++s) {
#pragma unroll
    for (int jj = 0; jj < 8; ++jj) loadw(s, jj);
    if (!XLDS)
#pragma unroll
      for (int t = 0; t < 3; ++t) loadx(s, t);
  }
  BAR();
  QSTAMP(0);
  if (XLDS)
#pragma unroll
    for (int t = 0; t < 3; ++t) loadx(0, t);
#pragma unroll
  for (int t = 0; t < 4; ++t) tr(0, t);
#pragma unroll
  for (int s = 0; s < 16; ++s)
#pragma unroll
  for (int e = 0; e < 8; ++e) {
    const int n = 8 * s + e;
    if (n == 8 * FILL_STEP + 4) BAR();
    SB();
    acc[0][e] = mfma16(f[n & 1], xb[s % (XA + 1)][0], acc[0][e]);
    if (n + 1 < 128) { tr(n + 1, 0); tr(n + 1, 1); }
    SB();
    acc[1][e] = mfma16(f[n & 1], xb[s % (XA + 1)][1], acc[1][e]);
    if (n + 1 < 128) { tr(n + 1, 2); tr(n + 1, 3); }
    SB();
    acc[2][e] = mfma16(f[n & 1], xb[s % (XA + 1)][2], acc[2][e]);
    if (e < 4 && s + 2 < 16) { loadw(s + 2, 2 * e); loadw(s + 2, 2 * e + 1); }
    if (e >= 4 && e < 7 && s + XA < 16) loadx(s + XA, e - 4);
    if (n == 7) QSTAMP(1);
    if (n == 63) QSTAMP(2);
  }
  SB();
#pragma unroll
  for (int t = 0; t < 3; ++t)
#pragma unroll
    for (int s = 0; s < 4; ++s)
#pragma unroll
      for (int e = 0; e < 4; ++e) qf[t][s][e] = pack2(acc[t][2 * e][s], acc[t][2 * e + 1][s]);
}

__device__ __forceinline__ void store_tile(char* op, const f32x4 (&o)[8], const f32x4& l) {
  const float inv = 1.0f / l[0];
#pragma unroll
  for (int m = 0; m < 4; ++m) {
    u32x4 f;
    f[0] = pack2(o[2 * m][0] * inv, o[2 * m][1] * inv);
    f[1] = pack2(o[2 * m][2] * inv, o[2 * m][3] * inv);
    f[2] = pack2(o[2 * m + 1][0] * inv, o[2 * m + 1][1] * inv);
    f[3] = pack2(o[2 * m + 1][2] * inv, o[2 * m + 1][3] * inv);
    asm volatile("global_store_dwordx4 %0, %1, off sc0 sc1\n\ts_nop 2" ::"v"(op + m * 1024), "v"(f) : "memory");
  }
}

__global__ __launch_bounds__(512, 2) void attn_kernel(AttnArgs a) {
  extern __shared__ __attribute__((aligned(16))) char smem[];
  const int lane = threadIdx.x & 63;
  const int w = __builtin_amdgcn_readfirstlane(threadIdx.x >> 6);
  const int xcd = blockIdx.x & 7, j = blockIdx.x >> 3;
  const bool type1 = j < 16;
  const int b = j & 15;
  const unsigned lds0 = (unsigned)(uintptr_t)(LAS char*)smem;
  const int nst = type1 ? 2 : 3, rdepth = type1 ? 5 : 3;
  const unsigned xoff = type1 ? XOFF1 : XOFF2;

  auto warm_wo = [&](int nbar, int q0, int q1) {
    const rsrc_t rw = mkrs(a.wo[xcd & 1], HD * DM * 2);
    const unsigned vo = (unsigned)((256 * b + (lane >> 2)) * (DM * 2) + (xcd >> 2) * 512 + (lane & 3) * 128);
    unsigned t[16];
#pragma unroll
    for (int q = 0; q < 16; ++q) t[q] = (q >= q0 && q < q1) ? __builtin_amdgcn_raw_buffer_load_b32(rw, vo, q * 16 * (DM * 2), 0) : 0u;
    for (int i = 0; i < nbar; ++i) BAR();
#pragma unroll
    for (int q = 0; q < 16; ++q) asm volatile("" ::"v"(t[q]));
  };
  if (w >= (type1 ? 5 : 4)) {
    const unsigned g = lane >> 4, r = lane & 15;
    unsigned tq[8];
#pragma unroll
    for (int q = 0; q < 8; ++q) tq[q] = 0;
    if (type1 && w == 5) {
      const unsigned vo = (unsigned)((32 * b + (lane >> 3)) * (HD * 2) + xcd * 1024 + (lane & 7) * 128);
      const unsigned v1 = vo + 8u * HD * 2, v2 = vo + 16u * HD * 2, v3 = vo + 24u * HD * 2;
      asm volatile(
          "s_nop 4\n\t"
          "global_load_dword %0, %8, %13\n\tglobal_load_dword %1, %9, %13\n\tglobal_load_dword %2, %10, %13\n\tglobal_load_dword %3, %11, %13\n\t"
          "global_load_dword %4, %8, %12\n\tglobal_load_dword %5, %9, %12\n\tglobal_load_dword %6, %10, %12\n\tglobal_load_dword %7, %11, %12"
          : "=&v"(tq[0]), "=&v"(tq[1]), "=&v"(tq[2]), "=&v"(tq[3]), "=&v"(tq[4]), "=&v"(tq[5]), "=&v"(tq[6]), "=&v"(tq[7])
          : "v"(vo), "v"(v1), "v"(v2), "v"(v3), "s"(a.wq[0]), "s"(a.wq[1])
          : "memory");
    }
    const int li = 7 - w;
    {
      const char* xg = a.x[type1 ? 0 : 1] + (size_t)b * (SQ * DM * 2);
      const int nq = type1 ? 4 : 3;
      for (int q = 0; q < nq; ++q) {
        const int rg = li * nq + q;
        const unsigned base = g * 256 + 16 * (r ^ (unsigned)(rg & 3));
        dma4c(xg + rg * 4096, lds0 + xoff + rg * 4096, base, base ^ 64u, base ^ 128u, base ^ 192u);
      }
    }
    const int nu = (type1 && li == 2) ? 2 : 3;
    const char* up[3];
    unsigned ul[3], uo[3][4];
    bool uA[3];
#pragma unroll
    for (int u = 0; u < 3; ++u) {
      const int id = (li * 3 + u) & (type1 ? 7 : 15);
      const int st = id >> 2, isv = (id >> 1) & 1, hf = id & 1;
      uA[u] = type1 && st == 1;
      const size_t sidx = uA[u] ? (size_t)(b * 8 + xcd) : (size_t)(b * 32 + 4 * xcd + (type1 ? 0 : 1 + st));
      const char* kb = uA[u] ? a.k[0] : a.k[1];
      const char* vb = uA[u] ? a.v[0] : a.v[1];
      up[u] = (isv ? vb : kb) + sidx * (SK * DH * 2) + hf * 4096;
      ul[u] = lds0 + st * BLK_BYTES + isv * 8192 + hf * 4096;
#pragma unroll
      for (int q = 0; q < 4; ++q) uo[u][q] = g * 256 + 16 * (r ^ (isv ? (((q & 1) * 4 + g) << 1) : ((g << 2) | q)));
    }
    const unsigned sstride = nst * BLK_BYTES;
    auto issue = [&](int blk, int slot, int which) {
#pragma unroll
      for (int u = 0; u < 3; ++u)
        if (u < nu && (which == 0 || (which == 2) == uA[u])) dma4(up[u] + blk * 8192, ul[u] + slot * sstride, uo[u][0], uo[u][1], uo[u][2], uo[u][3]);
    };
    int slot = rdepth - 1;
    if (type1) {
      issue(0, 0, 0); issue(1, 1, 0); issue(2, 2, 0);
      u32x4 rb[NRB][3][4];
#pragma unroll
      for (int u = 0; u < 3; ++u) {
        const rsrc_t rs = mkrs(up[u < nu ? u : 0], SK * DH * 2 - 4096);
#pragma unroll
        for (int k = 0; k < NRB; ++k)
#pragma unroll
          for (int q = 0; q < 4; ++q)
            rb[k][u][q] = (u < nu) ? __builtin_amdgcn_raw_buffer_load_b128(rs, uo[u][q] + q * 1024, (RB0 + k * RBS) * 8192, 2) : u32x4{0, 0, 0, 0};
      }
      asm volatile("s_waitcnt vmcnt(%0)" ::"n"(8 * NRB) : "memory");
      BAR();
      BAR();
      WAIT_VM(0);
#pragma unroll
      for (int k = 0; k < NRB; ++k)
#pragma unroll
        for (int u = 0; u < 3; ++u) asm volatile("" : "+v"(rb[k][u][0]), "+v"(rb[k][u][1]), "+v"(rb[k][u][2]), "+v"(rb[k][u][3]));
      auto isreg = [&](int blk) { return blk >= RB0 && blk < RB0 + NRB * RBS && (blk - RB0) % RBS == 0; };
      u32x4 q1[4];
      f32x4 o1[8], ls = f32x4{0.f, 0.f, 0.f, 0.f};
#pragma unroll
      for (int c = 0; c < 8; ++c) o1[c] = f32x4{0.f, 0.f, 0.f, 0.f};
      float mr = -__builtin_inff(), th = -__builtin_inff();
      u32x4 ones = {0x3f803f80u, 0x3f803f80u, 0x3f803f80u, 0x3f803f80u};
      asm volatile("" : "+v"(ones));
      const unsigned fk = ((r & 3) << 2) | (r >> 2), vrow = 4 * g + (r >> 2);
      const unsigned vra = 8192 + vrow * 256 + 8 * (r & 1), vsw = (vrow & 7) << 1, vp = (r >> 1) & 1;
      int cs = 0;
      for (int i = 0; i < NBLK; ++i) {
        int cnt = (i == 3) ? -1 : 0;
#pragma unroll
        for (int d = 1; d <= 3; ++d) cnt += (i + d < NBLK && !isreg(i + d)) ? 1 : 0;
        if (nu == 3) { if (cnt == 3) WAIT_VM(36); else if (cnt == 2) WAIT_VM(24); else if (cnt == 1) WAIT_VM(12); else WAIT_VM(0); }
        else { if (cnt == 3) WAIT_VM(24); else if (cnt == 2) WAIT_VM(16); else if (cnt == 1) WAIT_VM(8); else WAIT_VM(0); }
        BAR();
        if (i == 0) {
#pragma unroll
          for (int c = 0; c < 4; ++c) q1[c] = *(const LAS u32x4*)((LAS char*)smem + QOFF + (li * 4 + c) * 1024 + lane * 16);
          issue(3, 3, 2);
        }
        if (i == 1) issue(3, 3, 1);
        if (isreg(i + 4)) {
#pragma unroll
          for (int k = 0; k < NRB; ++k)
            if (i + 4 == RB0 + k * RBS) {
#pragma unroll
              for (int u = 0; u < 3; ++u)
                if (u < nu)
#pragma unroll
                  for (int q = 0; q < 4; ++q)
                    *(LAS u32x4*)(uintptr_t)(ul[u] + slot * sstride + q * 1024 + lane * 16) = rb[k][u][q];
            }
          WAIT_LGKM0();
        } else if (i + 4 < NBLK) {
          issue(i + 4, slot, 0);
        }
        slot = (slot + 1 == rdepth) ? 0 : slot + 1;
        const unsigned sb = cs * sstride;
        f32x4 sc[2] = {f32x4{0.f, 0.f, 0.f, 0.f}, f32x4{0.f, 0.f, 0.f, 0.f}};
#pragma unroll
        for (int c = 0; c < 8; ++c) {
          const u32x4 kf = *(const LAS u32x4*)((LAS char*)smem + sb + r * 256 + 16 * ((4 * g + (c & 3)) ^ fk) + (c >> 2) * 4096);
          sc[c >> 2] = mfma16(kf, q1[c & 3], sc[c >> 2]);
        }
        u32x4 vf[8], pf;
#pragma unroll
        for (int c = 0; c < 8; ++c) {
          const unsigned ad = sb + vra + 16 * ((2 * c + vp) ^ vsw);
          s16x4 l0 = __builtin_amdgcn_ds_read_tr16_b64_v4i16((LAS s16x4*)((LAS char*)smem + ad));
          s16x4 l1 = __builtin_amdgcn_ds_read_tr16_b64_v4i16((LAS s16x4*)((LAS char*)smem + ad + 4096));
          u32x2 u0 = __builtin_bit_cast(u32x2, l0), u1 = __builtin_bit_cast(u32x2, l1);
          vf[c] = u32x4{u0[0], u0[1], u1[0], u1[1]};
        }
        float lm = max3(max3(sc[0][0], sc[0][1], sc[0][2]), sc[0][3], sc[1][0]);
        lm = __builtin_elementwise_maximum(max3(lm, sc[1][1], sc[1][2]), sc[1][3]);
        if (__builtin_expect(__any(!(lm <= th)), 0)) {
          const float mn = fmaxf(mr, xg_max(lm) * SCL);
          const float al = __builtin_amdgcn_exp2f(mr - mn);
          mr = mn;
          th = (mn + RESCALE_THR) * (1.0f / SCL);
          ls *= al;
#pragma unroll
          for (int c = 0; c < 8; ++c) o1[c] *= al;
        }
#pragma unroll
        for (int e = 0; e < 4; ++e)
          pf[e] = pack2(__builtin_amdgcn_exp2f(fmaf(sc[e >> 1][2 * (e & 1)], SCL, -mr)), __builtin_amdgcn_exp2f(fmaf(sc[e >> 1][2 * (e & 1) + 1], SCL, -mr)));
#pragma unroll
        for (int c = 0; c < 8; ++c) o1[c] = mfma16(vf[c], pf, o1[c]);
        ls = mfma16(ones, pf, ls);
        cs = (cs + 1 == rdepth) ? 0 : cs + 1;
        WAIT_LGKM0();
      }
      store_tile(a.o + ((size_t)(512 + b * 32 + 4 * xcd) * 12 + li * 4) * 1024 + lane * 16, o1, ls);
    } else {
      WAIT_VM(0);
      BAR();
      BAR();
      issue(0, 0, 0); issue(1, 1, 0);
      for (int i = 0; i < NBLK; ++i) {
        if (i + 1 < NBLK) WAIT_VM(12); else WAIT_VM(0);
        BAR();
        if (i + 2 < NBLK) issue(i + 2, slot, 0);
        slot = (slot + 1 == rdepth) ? 0 : slot + 1;
      }
    }
#pragma unroll
    for (int q = 0; q < 8; ++q) asm volatile("" ::"v"(tq[q]));
    return;
  }
  if (!type1 && w == 3) {
    for (int i = 0; i < WO_PF_IT + 2; ++i) BAR();
    warm_wo(NBLK - WO_PF_IT, 0, 16);
    return;
  }

  const bool roleA = type1 && w < 4;
  const int p = roleA ? 0 : 1;
  const int h = roleA ? 4 * xcd + w : (type1 ? 4 * xcd : 4 * xcd + 1 + w);
  const unsigned ringoff = (unsigned)((type1 ? (roleA ? 1 : 0) : w) * BLK_BYTES);
  const bool xlds = !(type1 && w == 4);

  u32x4 qf[3][4];
  if (xlds) qproj<true>(qf, a.wq[p] + h * 256, DM * HD * 2 - h * 256, a.x[p] + b * (SQ * DM * 2), smem, xoff, lane);
  else qproj<false>(qf, a.wq[p] + h * 256, DM * HD * 2 - h * 256, a.x[p] + b * (SQ * DM * 2), smem, xoff, lane);
  if (!xlds) {
#pragma unroll
    for (int c = 0; c < 12; ++c) *(LAS u32x4*)((LAS char*)smem + QOFF + c * 1024 + lane * 16) = qf[c >> 2][c & 3];
    WAIT_LGKM0();
    BAR();
    return;
  }

  int lane2 = lane;
  asm volatile("" : "+v"(lane2));
  const int g = lane2 >> 4, r = lane2 & 15;
  f32x4 oacc[3][8];
#pragma unroll
  for (int t = 0; t < 3; ++t)
#pragma unroll
    for (int c = 0; c < 8; ++c) oacc[t][c] = f32x4{0.f, 0.f, 0.f, 0.f};
  float mrow[3] = {-__builtin_inff(), -__builtin_inff(), -__builtin_inff()};
  float thr[3] = {-__builtin_inff(), -__builtin_inff(), -__builtin_inff()};
  f32x4 lsum[3] = {f32x4{0.f, 0.f, 0.f, 0.f}, f32x4{0.f, 0.f, 0.f, 0.f}, f32x4{0.f, 0.f, 0.f, 0.f}};
  u32x4 ones = {0x3f803f80u, 0x3f803f80u, 0x3f803f80u, 0x3f803f80u};
  asm volatile("" : "+v"(ones));
  const unsigned fk = ((r & 3) << 2) | (r >> 2);
  unsigned kra[4];
#pragma unroll
  for (int s = 0; s < 4; ++s) kra[s] = r * 256 + 16 * ((4 * g + s) ^ fk);
  const unsigned vrow = 4 * g + (r >> 2);
  const unsigned vra = 8192 + vrow * 256 + 8 * (r & 1);
  const unsigned vsw = (vrow & 7) << 1, vp = (r >> 1) & 1;

  int slot = 0;
  for (int i = 0; i < NBLK; ++i) {
    const unsigned sb = ringoff + slot * nst * BLK_BYTES;
    WAIT_LGKM0();
    BAR();
    f32x4 sc[3][2];
    u32x4 kf[8], vf[8], pf[3];
    float lm = 0.f, pv[8];
#pragma unroll
    for (int s = 0; s < 8; ++s) kf[s] = *(const LAS u32x4*)((LAS char*)smem + sb + kra[s & 3] + (s >> 2) * 4096);
    auto qk = [&](int t, int s) {
      sc[t][s >> 2] = mfma16(kf[s], qf[t][s & 3], (s & 3) ? sc[t][s >> 2] : f32x4{0.f, 0.f, 0.f, 0.f});
    };
    auto ldv = [&](int c) {
      const unsigned ad = sb + vra + 16 * ((2 * c + vp) ^ vsw);
      s16x4 l0 = __builtin_amdgcn_ds_read_tr16_b64_v4i16((LAS s16x4*)((LAS char*)smem + ad));
      s16x4 l1 = __builtin_amdgcn_ds_read_tr16_b64_v4i16((LAS s16x4*)((LAS char*)smem + ad + 4096));
      u32x2 u0 = __builtin_bit_cast(u32x2, l0), u1 = __builtin_bit_cast(u32x2, l1);
      vf[c] = u32x4{u0[0], u0[1], u1[0], u1[1]};
    };
    auto mx = [&](int t, int q) {
      if (q == 0) lm = max3(sc[t][0][0], sc[t][0][1], sc[t][0][2]);
      else if (q == 1) lm = max3(lm, sc[t][0][3], sc[t][1][0]);
      else if (q == 2) lm = max3(lm, sc[t][1][1], sc[t][1][2]);
      else lm = __builtin_elementwise_maximum(lm, sc[t][1][3]);
    };
    auto rescale = [&](int t) {
      if (__builtin_expect(__any(!(lm <= thr[t])), 0)) {
        const float mn = fmaxf(mrow[t], xg_max(lm) * SCL);
        const float al = __builtin_amdgcn_exp2f(mrow[t] - mn);
        mrow[t] = mn;
        thr[t] = (mn + RESCALE_THR) * (1.0f / SCL);
        lsum[t] *= al;
#pragma unroll
        for (int c = 0; c < 8; ++c) oacc[t][c] *= al;
      }
    };
    auto ex = [&](int t, int e) {
      pv[e] = __builtin_amdgcn_exp2f(fmaf(sc[t][e >> 2][e & 3], SCL, -mrow[t]));
      if (e & 1) {
        unsigned u = pack2(pv[e - 1], pv[e]);
        asm volatile("" : "+v"(u));
        pf[t][e >> 1] = u;
      } else {
        asm volatile("" : "+v"(pv[e]));
      }
    };
    auto pvm = [&](int t, int c) {
      if (c < 8) oacc[t][c] = mfma16(vf[c], pf[t], oacc[t][c]);
      else lsum[t] = mfma16(ones, pf[t], lsum[t]);
    };
    SB();
#pragma unroll
    for (int s = 0; s < 8; ++s) qk(0, s);
    SB();
#pragma unroll
    for (int s = 0; s < 8; ++s) {
      qk(1, s);
      if (s >= 2 && s < 6) mx(0, s - 2);
      SB();
    }
    rescale(0);
#pragma unroll
    for (int s = 0; s < 8; ++s) {
      qk(2, s);
      ldv(s);
      if (s & 1) ex(0, s >> 1);
      SB();
    }
#pragma unroll
    for (int s = 0; s < 4; ++s) { ex(0, 4 + s); mx(1, s); SB(); }
    rescale(1);
#pragma unroll
    for (int s = 0; s < 9; ++s) {
      pvm(0, s);
      if (s < 8) ex(1, s);
      if (s >= 5) mx(2, s - 5);
      SB();
    }
    rescale(2);
#pragma unroll
    for (int s = 0; s < 9; ++s) {
      pvm(1, s);
      if (s < 8) ex(2, s);
      SB();
    }
#pragma unroll
    for (int s = 0; s < 9; ++s) pvm(2, s);
    slot = (slot + 1 == rdepth) ? 0 : slot + 1;
  }

#pragma unroll
  for (int t = 0; t < 3; ++t) store_tile(a.o + ((size_t)(p * 512 + b * 32 + h) * 12 + t * 4) * 1024 + lane2 * 16, oacc[t], lsum[t]);
}

struct OutArgs {
  const char* wo[2];
  const char* o;
  char* out;
};
#define OP_PHASE 0
constexpr int OPD = 5;
constexpr int LDS_OPROJ = 8 * OPD * 4096;
__global__ __launch_bounds__(512, 2) void oproj_kernel(OutArgs a) {
  extern __shared__ __attribute__((aligned(16))) char smem[];
  const int lane = threadIdx.x & 63;
  const int w = __builtin_amdgcn_readfirstlane(threadIdx.x >> 6);
  const int xcd = blockIdx.x & 7, j = blockIdx.x >> 3;
  const int p = xcd & 1, b = ((xcd >> 1) & 1) * 8 + (j & 7), nb = (xcd >> 2) * 4 + (j >> 3);
  const int g = lane >> 4, r = lane & 15;
  const int ph = OP_PHASE == 1 ? (2 * (j & 7) + 4 * (j >> 3)) : OP_PHASE == 2 ? 4 * (j >> 3) : OP_PHASE == 3 ? 8 * (j >> 4) : 0;
  const char* wg = a.wo[p] + (size_t)w * 512 * (DM * 2) + nb * 128;
  const char* og = a.o + (size_t)(p * 512 + b * 32 + 4 * w) * 12288;
  const unsigned lw = (unsigned)(uintptr_t)(LAS char*)smem + w * (OPD * 4096);
  const unsigned so = (lane >> 3) * (DM * 2) + 16 * ((lane & 7) ^ (g << 1));
  const unsigned so1 = so + 7168, so2 = so + 2 * 7168, so3 = so + 3 * 7168, ov = lane * 16;
  const unsigned trow = 4 * g + (r >> 2);
  const unsigned tra = w * (OPD * 4096) + trow * 128 + 8 * (r & 1), tsw = ((trow >> 1) & 3) << 1, tp = (r >> 1) & 1;
  f32x4 acc[3][4];
#pragma unroll
  for (int t = 0; t < 3; ++t)
#pragma unroll
    for (int e = 0; e < 4; ++e) acc[t][e] = f32x4{0.f, 0.f, 0.f, 0.f};
  u32x4 ob[OPD][3];
  auto load = [&](int s) {
    const int q = (s + ph) & 15;
    dma4c(wg + q * (32 * DM * 2), lw + (s % OPD) * 4096, so, so1, so2, so3);
    const char* os = og + (q >> 2) * 12288 + (q & 3) * 1024 + 4096;
    asm volatile(
        "global_load_dwordx4 %0, %3, %4 offset:-4096\n\tglobal_load_dwordx4 %1, %3, %4\n\tglobal_load_dwordx4 %2, %3, %5"
        : "=&v"(ob[s % OPD][0]), "=&v"(ob[s % OPD][1]), "=&v"(ob[s % OPD][2])
        : "v"(ov), "s"(os), "s"(os + 4096)
        : "memory");
  };
#pragma unroll
  for (int s = 0; s < OPD; ++s) load(s);
#pragma unroll
  for (int s = 0; s < 16; ++s) {
    const int left = (15 - s < OPD - 1) ? 15 - s : OPD - 1;
    asm volatile("s_waitcnt vmcnt(%3)" : "+v"(ob[s % OPD][0]), "+v"(ob[s % OPD][1]), "+v"(ob[s % OPD][2]) : "n"(7 * left) : "memory");
    u32x4 f[4];
#pragma unroll
    for (int e = 0; e < 4; ++e) {
      const unsigned ad = tra + (s % OPD) * 4096 + 16 * ((2 * e + tp) ^ tsw);
      s16x4 l0 = __builtin_amdgcn_ds_read_tr16_b64_v4i16((LAS s16x4*)((LAS char*)smem + ad));
      s16x4 l1 = __builtin_amdgcn_ds_read_tr16_b64_v4i16((LAS s16x4*)((LAS char*)smem + ad + 2048));
      u32x2 u0 = __builtin_bit_cast(u32x2, l0), u1 = __builtin_bit_cast(u32x2, l1);
      f[e] = u32x4{u0[0], u0[1], u1[0], u1[1]};
    }
#pragma unroll
    for (int e = 0; e < 4; ++e)
#pragma unroll
      for (int t = 0; t < 3; ++t) acc[t][e] = mfma16(f[e], ob[s % OPD][t], acc[t][e]);
    if (s + OPD < 16) {
      asm volatile("s_waitcnt lgkmcnt(0)" : "+v"(acc[2][3]) : : "memory");
      load(s + OPD);
    }
  }
  f32x4* red = (f32x4*)smem;
  constexpr int RS = OPD * 4096 / 16;
#pragma unroll
  for (int t = 0; t < 3; ++t)
#pragma unroll
    for (int e = 0; e < 4; ++e) red[w * RS + (t * 4 + e) * 64 + (((lane & 15) << 2) | (lane >> 4))] = acc[t][e];
  __syncthreads();
  if (w < 6) {
    const int t = w >> 1, e = (w & 1) * 2 + (lane & 2 ? 1 : 0), q = lane >> 2, gh = lane & 1;
    f32x4 s0 = f32x4{0.f, 0.f, 0.f, 0.f}, s1 = s0;
#pragma unroll
    for (int ww = 0; ww < 8; ++ww) {
      s0 += red[ww * RS + (t * 4 + e) * 64 + q * 4 + 2 * gh];
      s1 += red[ww * RS + (t * 4 + e) * 64 + q * 4 + 2 * gh + 1];
    }
    u32x4 f = {pack2(s0[0], s0[1]), pack2(s0[2], s0[3]), pack2(s1[0], s1[1]), pack2(s1[2], s1[3])};
    *(u32x4*)(a.out + ((size_t)(b * SQ + 16 * t + q) * 1024 + p * 512 + nb * 64 + 16 * e + 8 * gh) * 2) = f;
  }
}

extern "C" void kernel_launch(void* const* d_in, const int* in_sizes, int n_in, void* d_out, int out_size, void* d_ws,
                              size_t ws_size, hipStream_t stream) {
  (void)in_sizes; (void)n_in; (void)out_size; (void)ws_size;
  AttnArgs a;
  a.x[0] = (const char*)d_in[0]; a.k[0] = (const char*)d_in[1]; a.v[0] = (const char*)d_in[2]; a.wq[0] = (const char*)d_in[3];
  a.x[1] = (const char*)d_in[5]; a.k[1] = (const char*)d_in[6]; a.v[1] = (const char*)d_in[7]; a.wq[1] = (const char*)d_in[8];
  a.wo[0] = (const char*)d_in[4]; a.wo[1] = (const char*)d_in[9];
  a.o = (char*)d_ws;
  OutArgs oa;
  oa.wo[0] = (const char*)d_in[4]; oa.wo[1] = (const char*)d_in[9]; oa.o = (const char*)d_ws; oa.out = (char*)d_out;
  (void)hipFuncSetAttribute((const void*)attn_kernel, hipFuncAttributeMaxDynamicSharedMemorySize, LDS_ATTN);
  (void)hipFuncSetAttribute((const void*)oproj_kernel, hipFuncAttributeMaxDynamicSharedMemorySize, LDS_OPROJ);
  hipLaunchKernelGGL(attn_kernel, dim3(256), dim3(512), LDS_ATTN, stream, a);
  hipLaunchKernelGGL(oproj_kernel, dim3(256), dim3(512), LDS_OPROJ, stream, oa);
}
```

```cpp
#include <hip/hip_runtime.h>
#include <hip/hip_bf16.h>
#include <stdint.h>

typedef __attribute__((ext_vector_type(8))) __bf16 bf16x8;
typedef __attribute__((ext_vector_type(2))) __bf16 bf16x2;
typedef __attribute__((ext_vector_type(4))) short s16x4;
typedef __attribute__((ext_vector_type(4))) float f32x4;
typedef __attribute__((ext_vector_type(2))) float f32x2;
typedef __attribute__((ext_vector_type(4))) unsigned u32x4;
typedef __attribute__((ext_vector_type(2))) unsigned u32x2;
#define LAS __attribute__((address_space(3)))

constexpr int SQ = 48, DM = 512, NH = 32, SK = 1024, DH = 128, HD = NH * DH;
constexpr int NBLK = SK / 32;
constexpr int BLK_BYTES = 16384;
constexpr int LDS_ATTN = 10 * BLK_BYTES;
constexpr int QOFF = 6 * BLK_BYTES;
constexpr int XOFF1 = 7 * BLK_BYTES, XOFF2 = 6 * BLK_BYTES;
constexpr int FILL_STEP = 13;
constexpr int NRB = 2, RB0 = 8, RBS = 10;
constexpr float SCL = 0.088388347648318447f * 1.4426950408889634f;
constexpr float RESCALE_THR = 6.0f;

__device__ __forceinline__ unsigned pack2(float lo, float hi) {
  f32x2 f = {lo, hi};
  return __builtin_bit_cast(unsigned, __builtin_convertvector(f, bf16x2));
}
__device__ __forceinline__ f32x4 mfma16(u32x4 a, u32x4 b, f32x4 c) {
  return __builtin_amdgcn_mfma_f32_16x16x32_bf16(__builtin_bit_cast(bf16x8, a), __builtin_bit_cast(bf16x8, b), c, 0, 0, 0);
}
__device__ __forceinline__ float xg_max(float v) {
  auto r = __builtin_amdgcn_permlane16_swap(__float_as_uint(v), __float_as_uint(v), false, false);
  v = fmaxf(__uint_as_float(r[0]), __uint_as_float(r[1]));
  auto s = __builtin_amdgcn_permlane32_swap(__float_as_uint(v), __float_as_uint(v), false, false);
  return fmaxf(__uint_as_float(s[0]), __uint_as_float(s[1]));
}
__device__ __forceinline__ float max3(float a, float b, float c) {
  return __builtin_elementwise_maximum(__builtin_elementwise_maximum(a, b), c);
}
#define SB() __builtin_amdgcn_sched_barrier(0)

#define DMA4_BODY(POL)                                                        \
  unsigned keep;                                                              \
  asm volatile(                                                               \
      "s_nop 4\n\ts_mov_b32 %0, m0\n\ts_mov_b32 m0, %1\n\ts_nop 0\n\t"      \
      "global_load_lds_dwordx4 %3, %2" POL "\n\t"                             \
      "global_load_lds_dwordx4 %4, %2 offset:1024" POL "\n\t"                 \
      "global_load_lds_dwordx4 %5, %2 offset:2048" POL "\n\t"                 \
      "global_load_lds_dwordx4 %6, %2 offset:3072" POL "\n\t"                 \
      "s_mov_b32 m0, %0"                                                      \
      : "=&s"(keep)                                                           \
      : "s"(m), "s"(g), "v"(o0), "v"(o1), "v"(o2), "v"(o3)                    \
      : "memory")
__device__ __forceinline__ void dma4(const char* g, unsigned m, unsigned o0, unsigned o1, unsigned o2, unsigned o3) { DMA4_BODY(" nt"); }
__device__ __forceinline__ void dma4c(const char* g, unsigned m, unsigned o0, unsigned o1, unsigned o2, unsigned o3) { DMA4_BODY(""); }
#define WAIT_VM(n) asm volatile("s_waitcnt vmcnt(" #n ")" ::: "memory")
#define WAIT_LGKM0() asm volatile("s_waitcnt lgkmcnt(0)" ::: "memory")
#define BAR() asm volatile("s_barrier" ::: "memory")

typedef __amdgpu_buffer_rsrc_t rsrc_t;
__device__ __forceinline__ rsrc_t mkrs(const void* p, int bytes) { return __builtin_amdgcn_make_buffer_rsrc((void*)p, (short)0, bytes, 0x00020000); }
__device__ __forceinline__ u32x4 bld128(rsrc_t rs, unsigned vo, unsigned so) { return __builtin_amdgcn_raw_buffer_load_b128(rs, vo, so, 0); }

struct AttnArgs {
  const char* x[2];
  const char* k[2];
  const char* v[2];
  const char* wq[2];
  const char* wo[2];
  char* o;
};

template <bool XLDS>
__device__ __forceinline__ void qproj(u32x4 (&qf)[3][4], const char* wq, int wbytes, const char* x, char* smem, unsigned xoff, int lane) {
  f32x4 acc[3][8];
#pragma unroll
  for (int t = 0; t < 3; ++t)
#pragma unroll
    for (int e = 0; e < 8; ++e) acc[t][e] = f32x4{0.f, 0.f, 0.f, 0.f};
  const rsrc_t rw = mkrs(wq, wbytes);
  const rsrc_t rx = mkrs(x, SQ * DM * 2);
  const unsigned g = lane >> 4, r = lane & 15;
  const unsigned wv = g * (32 * HD * 2) + r * 16;
  const unsigned xv = r * (DM * 2) + g * 64;
  const unsigned xl = xoff + r * (DM * 2) + 16 * ((4 * g) ^ (((r & 3) << 2) | (r >> 2)));
  constexpr int XA = XLDS ? 1 : 2;
  u32x4 wb[3][8], xb[XA + 1][3];
  auto loadw = [&](int s, int jj) { wb[s % 3][jj] = bld128(rw, wv, (128 * (s >> 2) + 8 * (s & 3) + jj) * (HD * 2)); };
  auto loadx = [&](int s, int t) {
    if (XLDS) xb[s % (XA + 1)][t] = *(const LAS u32x4*)((LAS char*)smem + (xl ^ (16 * (s & 3))) + t * 16 * DM * 2 + (s >> 2) * 256);
    else xb[s % (XA + 1)][t] = bld128(rx, xv + (s >> 2) * 256 + (s & 3) * 16, t * 16 * DM * 2);
  };
  u32x4 f[2];
  auto tr = [&](int n, int t) {
    const int s = n >> 3, e = n & 7;
    f[n & 1][t] = __builtin_amdgcn_perm(wb[s % 3][2 * t + 1][e >> 1], wb[s % 3][2 * t][e >> 1], (e & 1) ? 0x07060302u : 0x05040100u);
  };
  const bool late = xoff == XOFF1;
  if (late) BAR();
#pragma unroll
  for (int s = 0; s < 2; ++s) {
#pragma unroll
    for (int jj = 0; jj < 8; ++jj) loadw(s, jj);
    if (!XLDS)
#pragma unroll
      for (int t = 0; t < 3; ++t) loadx(s, t);
  }
  if (!late) BAR();
  if (XLDS)
#pragma unroll
    for (int t = 0; t < 3; ++t) loadx(0, t);
#pragma unroll
  for (int t = 0; t < 4; ++t) tr(0, t);
#pragma unroll
  for (int s = 0; s < 16; ++s)
#pragma unroll
  for (int e = 0; e < 8; ++e) {
    const int n = 8 * s + e;
    if (n == 8 * FILL_STEP + 4) BAR();
    SB();
    acc[0][e] = mfma16(f[n & 1], xb[s % (XA + 1)][0], acc[0][e]);
    if (n + 1 < 128) { tr(n + 1, 0); tr(n + 1, 1); }
    SB();
    acc[1][e] = mfma16(f[n & 1], xb[s % (XA + 1)][1], acc[1][e]);
    if (n + 1 < 128) { tr(n + 1, 2); tr(n + 1, 3); }
    SB();
    acc[2][e] = mfma16(f[n & 1], xb[s % (XA + 1)][2], acc[2][e]);
    if (e < 4 && s + 2 < 16) { loadw(s + 2, 2 * e); loadw(s + 2, 2 * e + 1); }
    if (e >= 4 && e < 7 && s + XA < 16) loadx(s + XA, e - 4);
  }
  SB();
#pragma unroll
  for (int t = 0; t < 3; ++t)
#pragma unroll
    for (int s = 0; s < 4; ++s)
#pragma unroll
      for (int e = 0; e < 4; ++e) qf[t][s][e] = pack2(acc[t][2 * e][s], acc[t][2 * e + 1][s]);
}

__device__ __forceinline__ void store_tile(char* op, const f32x4 (&o)[8], const f32x4& l) {
  const float inv = 1.0f / l[0];
#pragma unroll
  for (int m = 0; m < 4; ++m) {
    u32x4 f;
    f[0] = pack2(o[2 * m][0] * inv, o[2 * m][1] * inv);
    f[1] = pack2(o[2 * m][2] * inv, o[2 * m][3] * inv);
    f[2] = pack2(o[2 * m + 1][0] * inv, o[2 * m + 1][1] * inv);
    f[3] = pack2(o[2 * m + 1][2] * inv, o[2 * m + 1][3] * inv);
    asm volatile("global_store_dwordx4 %0, %1, off sc0 sc1\n\ts_nop 2" ::"v"(op + m * 1024), "v"(f) : "memory");
  }
}

__global__ __launch_bounds__(512, 2) void attn_kernel(AttnArgs a) {
  extern __shared__ __attribute__((aligned(16))) char smem[];
  const int lane = threadIdx.x & 63;
  const int w = __builtin_amdgcn_readfirstlane(threadIdx.x >> 6);
  const int xcd = blockIdx.x & 7, j = blockIdx.x >> 3;
  const bool type1 = j < 16;
  const int b = j & 15;
  const unsigned lds0 = (unsigned)(uintptr_t)(LAS char*)smem;
  const int nst = type1 ? 2 : 3, rdepth = type1 ? 5 : 3;
  const unsigned xoff = type1 ? XOFF1 : XOFF2;

  if (w >= (type1 ? 5 : 4)) {
    const unsigned g = lane >> 4, r = lane & 15;
    unsigned tq[8];
#pragma unroll
    for (int q = 0; q < 8; ++q) tq[q] = 0;
    if (type1 && w == 5) {
      const unsigned vo = (unsigned)((32 * b + (lane >> 3)) * (HD * 2) + xcd * 1024 + (lane & 7) * 128);
      const unsigned v1 = vo + 8u * HD * 2, v2 = vo + 16u * HD * 2, v3 = vo + 24u * HD * 2;
      asm volatile(
          "s_nop 4\n\t"
          "global_load_dword %0, %8, %13\n\tglobal_load_dword %1, %9, %13\n\tglobal_load_dword %2, %10, %13\n\tglobal_load_dword %3, %11, %13\n\t"
          "global_load_dword %4, %8, %12\n\tglobal_load_dword %5, %9, %12\n\tglobal_load_dword %6, %10, %12\n\tglobal_load_dword %7, %11, %12"
          : "=&v"(tq[0]), "=&v"(tq[1]), "=&v"(tq[2]), "=&v"(tq[3]), "=&v"(tq[4]), "=&v"(tq[5]), "=&v"(tq[6]), "=&v"(tq[7])
          : "v"(vo), "v"(v1), "v"(v2), "v"(v3), "s"(a.wq[0]), "s"(a.wq[1])
          : "memory");
    }
    const int li = 7 - w;
    {
      const char* xg = a.x[type1 ? 0 : 1] + (size_t)b * (SQ * DM * 2);
      const int nq = type1 ? 4 : 3;
      for (int q = 0; q < nq; ++q) {
        const int rg = li * nq + q;
        const unsigned base = g * 256 + 16 * (r ^ (unsigned)(rg & 3));
        dma4c(xg + rg * 4096, lds0 + xoff + rg * 4096, base, base ^ 64u, base ^ 128u, base ^ 192u);
      }
    }
    const int nu = (type1 && li == 2) ? 2 : 3;
    const char* up[3];
    unsigned ul[3], uo[3][4];
    bool uA[3];
#pragma unroll
    for (int u = 0; u < 3; ++u) {
      const int id = (li * 3 + u) & (type1 ? 7 : 15);
      const int st = id >> 2, isv = (id >> 1) & 1, hf = id & 1;
      uA[u] = type1 && st == 1;
      const size_t sidx = uA[u] ? (size_t)(b * 8 + xcd) : (size_t)(b * 32 + 4 * xcd + (type1 ? 0 : 1 + st));
      const char* kb = uA[u] ? a.k[0] : a.k[1];
      const char* vb = uA[u] ? a.v[0] : a.v[1];
      up[u] = (isv ? vb : kb) + sidx * (SK * DH * 2) + hf * 4096;
      ul[u] = lds0 + st * BLK_BYTES + isv * 8192 + hf * 4096;
#pragma unroll
      for (int q = 0; q < 4; ++q) uo[u][q] = g * 256 + 16 * (r ^ (isv ? (((q & 1) * 4 + g) << 1) : ((g << 2) | q)));
    }
    const unsigned sstride = nst * BLK_BYTES;
    auto issue = [&](int blk, int slot, int which) {
#pragma unroll
      for (int u = 0; u < 3; ++u)
        if (u < nu && (which == 0 || (which == 2) == uA[u])) dma4(up[u] + blk * 8192, ul[u] + slot * sstride, uo[u][0], uo[u][1], uo[u][2], uo[u][3]);
    };
    int slot = rdepth - 1;
    if (type1) {
      issue(0, 0, 0); issue(1, 1, 0); issue(2, 2, 0);
      u32x4 rb[NRB][3][4];
#pragma unroll
      for (int u = 0; u < 3; ++u) {
        const rsrc_t rs = mkrs(up[u < nu ? u : 0], SK * DH * 2 - 4096);
#pragma unroll
        for (int k = 0; k < NRB; ++k)
#pragma unroll
          for (int q = 0; q < 4; ++q)
            rb[k][u][q] = (u < nu) ? __builtin_amdgcn_raw_buffer_load_b128(rs, uo[u][q] + q * 1024, (RB0 + k * RBS) * 8192, 2) : u32x4{0, 0, 0, 0};
      }
      asm volatile("s_waitcnt vmcnt(%0)" ::"n"(8 * NRB) : "memory");
      BAR();
      BAR();
      WAIT_VM(0);
#pragma unroll
      for (int k = 0; k < NRB; ++k)
#pragma unroll
        for (int u = 0; u < 3; ++u) asm volatile("" : "+v"(rb[k][u][0]), "+v"(rb[k][u][1]), "+v"(rb[k][u][2]), "+v"(rb[k][u][3]));
      auto isreg = [&](int blk) { return blk >= RB0 && blk < RB0 + NRB * RBS && (blk - RB0) % RBS == 0; };
      u32x4 q1[4];
      f32x4 o1[8], ls = f32x4{0.f, 0.f, 0.f, 0.f};
#pragma unroll
      for (int c = 0; c < 8; ++c) o1[c] = f32x4{0.f, 0.f, 0.f, 0.f};
      float mr = -__builtin_inff(), th = -__builtin_inff();
      u32x4 ones = {0x3f803f80u, 0x3f803f80u, 0x3f803f80u, 0x3f803f80u};
      asm volatile("" : "+v"(ones));
      const unsigned fk = ((r & 3) << 2) | (r >> 2), vrow = 4 * g + (r >> 2);
      const unsigned vra = 8192 + vrow * 256 + 8 * (r & 1), vsw = (vrow & 7) << 1, vp = (r >> 1) & 1;
      int cs = 0;
      for (int i = 0; i < NBLK; ++i) {
        int cnt = (i == 3) ? -1 : 0;
#pragma unroll
        for (int d = 1; d <= 3; ++d) cnt += (i + d < NBLK && !isreg(i + d)) ? 1 : 0;
        if (nu == 3) { if (cnt == 3) WAIT_VM(36); else if (cnt == 2) WAIT_VM(24); else if (cnt == 1) WAIT_VM(12); else WAIT_VM(0); }
        else { if (cnt == 3) WAIT_VM(24); else if (cnt == 2) WAIT_VM(16); else if (cnt == 1) WAIT_VM(8); else WAIT_VM(0); }
        BAR();
        if (i == 0) {
#pragma unroll
          for (int c = 0; c < 4; ++c) q1[c] = *(const LAS u32x4*)((LAS char*)smem + QOFF + (li * 4 + c) * 1024 + lane * 16);
          issue(3, 3, 2);
        }
        if (i == 1) issue(3, 3, 1);
        if (isreg(i + 4)) {
#pragma unroll
          for (int k = 0; k < NRB; ++k)
            if (i + 4 == RB0 + k * RBS) {
#pragma unroll
              for (int u = 0; u < 3; ++u)
                if (u < nu)
#pragma unroll
                  for (int q = 0; q < 4; ++q)
                    *(LAS u32x4*)(uintptr_t)(ul[u] + slot * sstride + q * 1024 + lane * 16) = rb[k][u][q];
            }
          WAIT_LGKM0();
        } else if (i + 4 < NBLK) {
          issue(i + 4, slot, 0);
        }
        slot = (slot + 1 == rdepth) ? 0 : slot + 1;
        const unsigned sb = cs * sstride;
        f32x4 sc[2] = {f32x4{0.f, 0.f, 0.f, 0.f}, f32x4{0.f, 0.f, 0.f, 0.f}};
#pragma unroll
        for (int c = 0; c < 8; ++c) {
          const u32x4 kf = *(const LAS u32x4*)((LAS char*)smem + sb + r * 256 + 16 * ((4 * g + (c & 3)) ^ fk) + (c >> 2) * 4096);
          sc[c >> 2] = mfma16(kf, q1[c & 3], sc[c >> 2]);
        }
        u32x4 vf[8], pf;
#pragma unroll
        for (int c = 0; c < 8; ++c) {
          const unsigned ad = sb + vra + 16 * ((2 * c + vp) ^ vsw);
          s16x4 l0 = __builtin_amdgcn_ds_read_tr16_b64_v4i16((LAS s16x4*)((LAS char*)smem + ad));
          s16x4 l1 = __builtin_amdgcn_ds_read_tr16_b64_v4i16((LAS s16x4*)((LAS char*)smem + ad + 4096));
          u32x2 u0 = __builtin_bit_cast(u32x2, l0), u1 = __builtin_bit_cast(u32x2, l1);
          vf[c] = u32x4{u0[0], u0[1], u1[0], u1[1]};
        }
        float lm = max3(max3(sc[0][0], sc[0][1], sc[0][2]), sc[0][3], sc[1][0]);
        lm = __builtin_elementwise_maximum(max3(lm, sc[1][1], sc[1][2]), sc[1][3]);
        if (__builtin_expect(__any(!(lm <= th)), 0)) {
          const float mn = fmaxf(mr, xg_max(lm) * SCL);
          const float al = __builtin_amdgcn_exp2f(mr - mn);
          mr = mn;
          th = (mn + RESCALE_THR) * (1.0f / SCL);
          ls *= al;
#pragma unroll
          for (int c = 0; c < 8; ++c) o1[c] *= al;
        }
#pragma unroll
        for (int e = 0; e < 4; ++e)
          pf[e] = pack2(__builtin_amdgcn_exp2f(fmaf(sc[e >> 1][2 * (e & 1)], SCL, -mr)), __builtin_amdgcn_exp2f(fmaf(sc[e >> 1][2 * (e & 1) + 1], SCL, -mr)));
#pragma unroll
        for (int c = 0; c < 8; ++c) o1[c] = mfma16(vf[c], pf, o1[c]);
        ls = mfma16(ones, pf, ls);
        cs = (cs + 1 == rdepth) ? 0 : cs + 1;
        WAIT_LGKM0();
      }
      store_tile(a.o + ((size_t)(512 + b * 32 + 4 * xcd) * 12 + li * 4) * 1024 + lane * 16, o1, ls);
    } else {
      WAIT_VM(0);
      BAR();
      BAR();
      issue(0, 0, 0); issue(1, 1, 0);
      for (int i = 0; i < NBLK; ++i) {
        if (i + 1 < NBLK) WAIT_VM(12); else WAIT_VM(0);
        BAR();
        if (i + 2 < NBLK) issue(i + 2, slot, 0);
        slot = (slot + 1 == rdepth) ? 0 : slot + 1;
      }
    }
#pragma unroll
    for (int q = 0; q < 8; ++q) asm volatile("" ::"v"(tq[q]));
    return;
  }
  if (!type1 && w == 3) {
    for (int i = 0; i < NBLK + 2; ++i) BAR();
    const rsrc_t rw = mkrs(a.wo[xcd & 1], HD * DM * 2);
    const unsigned vo = (unsigned)((256 * b + (lane >> 2)) * (DM * 2) + (xcd >> 2) * 512 + (lane & 3) * 128);
    unsigned t[16];
#pragma unroll
    for (int q = 0; q < 16; ++q) t[q] = __builtin_amdgcn_raw_buffer_load_b32(rw, vo, q * 16 * (DM * 2), 0);
#pragma unroll
    for (int q = 0; q < 16; ++q) asm volatile("" ::"v"(t[q]));
    return;
  }

  const bool roleA = type1 && w < 4;
  const int p = roleA ? 0 : 1;
  const int h = roleA ? 4 * xcd + w : (type1 ? 4 * xcd : 4 * xcd + 1 + w);
  const unsigned ringoff = (unsigned)((type1 ? (roleA ? 1 : 0) : w) * BLK_BYTES);
  const bool xlds = !(type1 && w == 4);

  u32x4 qf[3][4];
  if (xlds) qproj<true>(qf, a.wq[p] + h * 256, DM * HD * 2 - h * 256, a.x[p] + b * (SQ * DM * 2), smem, xoff, lane);
  else qproj<false>(qf, a.wq[p] + h * 256, DM * HD * 2 - h * 256, a.x[p] + b * (SQ * DM * 2), smem, xoff, lane);
  if (!xlds) {
#pragma unroll
    for (int c = 0; c < 12; ++c) *(LAS u32x4*)((LAS char*)smem + QOFF + c * 1024 + lane * 16) = qf[c >> 2][c & 3];
    WAIT_LGKM0();
    BAR();
    return;
  }

  int lane2 = lane;
  asm volatile("" : "+v"(lane2));
  const int g = lane2 >> 4, r = lane2 & 15;
  f32x4 oacc[3][8];
#pragma unroll
  for (int t = 0; t < 3; ++t)
#pragma unroll
    for (int c = 0; c < 8; ++c) oacc[t][c] = f32x4{0.f, 0.f, 0.f, 0.f};
  float mrow[3] = {-__builtin_inff(), -__builtin_inff(), -__builtin_inff()};
  float thr[3] = {-__builtin_inff(), -__builtin_inff(), -__builtin_inff()};
  f32x4 lsum[3] = {f32x4{0.f, 0.f, 0.f, 0.f}, f32x4{0.f, 0.f, 0.f, 0.f}, f32x4{0.f, 0.f, 0.f, 0.f}};
  u32x4 ones = {0x3f803f80u, 0x3f803f80u, 0x3f803f80u, 0x3f803f80u};
  asm volatile("" : "+v"(ones));
  const unsigned fk = ((r & 3) << 2) | (r >> 2);
  unsigned kra[4];
#pragma unroll
  for (int s = 0; s < 4; ++s) kra[s] = r * 256 + 16 * ((4 * g + s) ^ fk);
  const unsigned vrow = 4 * g + (r >> 2);
  const unsigned vra = 8192 + vrow * 256 + 8 * (r & 1);
  const unsigned vsw = (vrow & 7) << 1, vp = (r >> 1) & 1;

  int slot = 0;
  for (int i = 0; i < NBLK; ++i) {
    const unsigned sb = ringoff + slot * nst * BLK_BYTES;
    WAIT_LGKM0();
    BAR();
    f32x4 sc[3][2];
    u32x4 kf[8], vf[8], pf[3];
    float lm = 0.f, pv[8];
#pragma unroll
    for (int s = 0; s < 8; ++s) kf[s] = *(const LAS u32x4*)((LAS char*)smem + sb + kra[s & 3] + (s >> 2) * 4096);
    auto qk = [&](int t, int s) {
      sc[t][s >> 2] = mfma16(kf[s], qf[t][s & 3], (s & 3) ? sc[t][s >> 2] : f32x4{0.f, 0.f, 0.f, 0.f});
    };
    auto ldv = [&](int c) {
      const unsigned ad = sb + vra + 16 * ((2 * c + vp) ^ vsw);
      s16x4 l0 = __builtin_amdgcn_ds_read_tr16_b64_v4i16((LAS s16x4*)((LAS char*)smem + ad));
      s16x4 l1 = __builtin_amdgcn_ds_read_tr16_b64_v4i16((LAS s16x4*)((LAS char*)smem + ad + 4096));
      u32x2 u0 = __builtin_bit_cast(u32x2, l0), u1 = __builtin_bit_cast(u32x2, l1);
      vf[c] = u32x4{u0[0], u0[1], u1[0], u1[1]};
    };
    auto mx = [&](int t, int q) {
      if (q == 0) lm = max3(sc[t][0][0], sc[t][0][1], sc[t][0][2]);
      else if (q == 1) lm = max3(lm, sc[t][0][3], sc[t][1][0]);
      else if (q == 2) lm = max3(lm, sc[t][1][1], sc[t][1][2]);
      else lm = __builtin_elementwise_maximum(lm, sc[t][1][3]);
    };
    auto rescale = [&](int t) {
      if (__builtin_expect(__any(!(lm <= thr[t])), 0)) {
        const float mn = fmaxf(mrow[t], xg_max(lm) * SCL);
        const float al = __builtin_amdgcn_exp2f(mrow[t] - mn);
        mrow[t] = mn;
        thr[t] = (mn + RESCALE_THR) * (1.0f / SCL);
        lsum[t] *= al;
#pragma unroll
        for (int c = 0; c < 8; ++c) oacc[t][c] *= al;
      }
    };
    auto ex = [&](int t, int e) {
      pv[e] = __builtin_amdgcn_exp2f(fmaf(sc[t][e >> 2][e & 3], SCL, -mrow[t]));
      if (e & 1) {
        unsigned u = pack2(pv[e - 1], pv[e]);
        asm volatile("" : "+v"(u));
        pf[t][e >> 1] = u;
      } else {
        asm volatile("" : "+v"(pv[e]));
      }
    };
    auto pvm = [&](int t, int c) {
      if (c < 8) oacc[t][c] = mfma16(vf[c], pf[t], oacc[t][c]);
      else lsum[t] = mfma16(ones, pf[t], lsum[t]);
    };
    SB();
#pragma unroll
    for (int s = 0; s < 8; ++s) qk(0, s);
    SB();
#pragma unroll
    for (int s = 0; s < 8; ++s) {
      qk(1, s);
      if (s >= 2 && s < 6) mx(0, s - 2);
      SB();
    }
    rescale(0);
#pragma unroll
    for (int s = 0; s < 8; ++s) {
      qk(2, s);
      ldv(s);
      if (s & 1) ex(0, s >> 1);
      SB();
    }
#pragma unroll
    for (int s = 0; s < 4; ++s) { ex(0, 4 + s); mx(1, s); SB(); }
    rescale(1);
#pragma unroll
    for (int s = 0; s < 9; ++s) {
      pvm(0, s);
      if (s < 8) ex(1, s);
      if (s >= 5) mx(2, s - 5);
      SB();
    }
    rescale(2);
#pragma unroll
    for (int s = 0; s < 9; ++s) {
      pvm(1, s);
      if (s < 8) ex(2, s);
      SB();
    }
#pragma unroll
    for (int s = 0; s < 9; ++s) pvm(2, s);
    slot = (slot + 1 == rdepth) ? 0 : slot + 1;
  }

#pragma unroll
  for (int t = 0; t < 3; ++t) store_tile(a.o + ((size_t)(p * 512 + b * 32 + h) * 12 + t * 4) * 1024 + lane2 * 16, oacc[t], lsum[t]);
}

struct OutArgs {
  const char* wo[2];
  const char* o;
  char* out;
};
constexpr int OPD = 5;
constexpr int LDS_OPROJ = 8 * OPD * 4096;
__global__ __launch_bounds__(512, 2) void oproj_kernel(OutArgs a) {
  extern __shared__ __attribute__((aligned(16))) char smem[];
  const int lane = threadIdx.x & 63;
  const int w = __builtin_amdgcn_readfirstlane(threadIdx.x >> 6);
  const int xcd = blockIdx.x & 7, j = blockIdx.x >> 3;
  const int p = xcd & 1, b = ((xcd >> 1) & 1) * 8 + (j & 7), nb = (xcd >> 2) * 4 + (j >> 3);
  const int g = lane >> 4, r = lane & 15;
  const char* wg = a.wo[p] + (size_t)w * 512 * (DM * 2) + nb * 128;
  const char* og = a.o + (size_t)(p * 512 + b * 32 + 4 * w) * 12288;
  const unsigned lw = (unsigned)(uintptr_t)(LAS char*)smem + w * (OPD * 4096);
  const unsigned so = (lane >> 3) * (DM * 2) + 16 * ((lane & 7) ^ (g << 1));
  const unsigned so1 = so + 7168, so2 = so + 2 * 7168, so3 = so + 3 * 7168, ov = lane * 16;
  const unsigned trow = 4 * g + (r >> 2);
  const unsigned tra = w * (OPD * 4096) + trow * 128 + 8 * (r & 1), tsw = ((trow >> 1) & 3) << 1, tp = (r >> 1) & 1;
  f32x4 acc[3][4];
#pragma unroll
  for (int t = 0; t < 3; ++t)
#pragma unroll
    for (int e = 0; e < 4; ++e) acc[t][e] = f32x4{0.f, 0.f, 0.f, 0.f};
  u32x4 ob[OPD][3];
  auto load = [&](int s) {
    dma4c(wg + s * (32 * DM * 2), lw + (s % OPD) * 4096, so, so1, so2, so3);
    const char* os = og + (s >> 2) * 12288 + (s & 3) * 1024 + 4096;
    asm volatile(
        "global_load_dwordx4 %0, %3, %4 offset:-4096\n\tglobal_load_dwordx4 %1, %3, %4\n\tglobal_load_dwordx4 %2, %3, %5"
        : "=&v"(ob[s % OPD][0]), "=&v"(ob[s % OPD][1]), "=&v"(ob[s % OPD][2])
        : "v"(ov), "s"(os), "s"(os + 4096)
        : "memory");
  };
#pragma unroll
  for (int s = 0; s < OPD; ++s) load(s);
#pragma unroll
  for (int s = 0; s < 16; ++s) {
    const int left = (15 - s < OPD - 1) ? 15 - s : OPD - 1;
    asm volatile("s_waitcnt vmcnt(%3)" : "+v"(ob[s % OPD][0]), "+v"(ob[s % OPD][1]), "+v"(ob[s % OPD][2]) : "n"(7 * left) : "memory");
    u32x4 f[4];
#pragma unroll
    for (int e = 0; e < 4; ++e) {
      const unsigned ad = tra + (s % OPD) * 4096 + 16 * ((2 * e + tp) ^ tsw);
      s16x4 l0 = __builtin_amdgcn_ds_read_tr16_b64_v4i16((LAS s16x4*)((LAS char*)smem + ad));
      s16x4 l1 = __builtin_amdgcn_ds_read_tr16_b64_v4i16((LAS s16x4*)((LAS char*)smem + ad + 2048));
      u32x2 u0 = __builtin_bit_cast(u32x2, l0), u1 = __builtin_bit_cast(u32x2, l1);
      f[e] = u32x4{u0[0], u0[1], u1[0], u1[1]};
    }
#pragma unroll
    for (int e = 0; e < 4; ++e)
#pragma unroll
      for (int t = 0; t < 3; ++t) acc[t][e] = mfma16(f[e], ob[s % OPD][t], acc[t][e]);
    if (s + OPD < 16) {
      asm volatile("s_waitcnt lgkmcnt(0)" : "+v"(acc[2][3]) : : "memory");
      load(s + OPD);
    }
  }
  f32x4* red = (f32x4*)smem;
  constexpr int RS = OPD * 4096 / 16;
#pragma unroll
  for (int t = 0; t < 3; ++t)
#pragma unroll
    for (int e = 0; e < 4; ++e) red[w * RS + (t * 4 + e) * 64 + (((lane & 15) << 2) | (lane >> 4))] = acc[t][e];
  __syncthreads();
  if (w < 6) {
    const int t = w >> 1, e = (w & 1) * 2 + (lane & 2 ? 1 : 0), q = lane >> 2, gh = lane & 1;
    f32x4 s0 = f32x4{0.f, 0.f, 0.f, 0.f}, s1 = s0;
#pragma unroll
    for (int ww = 0; ww < 8; ++ww) {
      s0 += red[ww * RS + (t * 4 + e) * 64 + q * 4 + 2 * gh];
      s1 += red[ww * RS + (t * 4 + e) * 64 + q * 4 + 2 * gh + 1];
    }
    u32x4 f = {pack2(s0[0], s0[1]), pack2(s0[2], s0[3]), pack2(s1[0], s1[1]), pack2(s1[2], s1[3])};
    *(u32x4*)(a.out + ((size_t)(b * SQ + 16 * t + q) * 1024 + p * 512 + nb * 64 + 16 * e + 8 * gh) * 2) = f;
  }
}

extern "C" void kernel_launch(void* const* d_in, const int* in_sizes, int n_in, void* d_out, int out_size, void* d_ws,
                              size_t ws_size, hipStream_t stream) {
  (void)in_sizes; (void)n_in; (void)out_size; (void)ws_size;
  AttnArgs a;
  a.x[0] = (const char*)d_in[0]; a.k[0] = (const char*)d_in[1]; a.v[0] = (const char*)d_in[2]; a.wq[0] = (const char*)d_in[3];
  a.x[1] = (const char*)d_in[5]; a.k[1] = (const char*)d_in[6]; a.v[1] = (const char*)d_in[7]; a.wq[1] = (const char*)d_in[8];
  a.wo[0] = (const char*)d_in[4]; a.wo[1] = (const char*)d_in[9];
  a.o = (char*)d_ws;
  OutArgs oa;
  oa.wo[0] = (const char*)d_in[4]; oa.wo[1] = (const char*)d_in[9]; oa.o = (const char*)d_ws; oa.out = (char*)d_out;
  (void)hipFuncSetAttribute((const void*)attn_kernel, hipFuncAttributeMaxDynamicSharedMemorySize, LDS_ATTN);
  (void)hipFuncSetAttribute((const void*)oproj_kernel, hipFuncAttributeMaxDynamicSharedMemorySize, LDS_OPROJ);
  hipLaunchKernelGGL(attn_kernel, dim3(256), dim3(512), LDS_ATTN, stream, a);
  hipLaunchKernelGGL(oproj_kernel, dim3(256), dim3(512), LDS_OPROJ, stream, oa);
}
```

```cpp
#include <hip/hip_runtime.h>
#include <hip/hip_bf16.h>
#include <stdint.h>

typedef __attribute__((ext_vector_type(8))) __bf16 bf16x8;
typedef __attribute__((ext_vector_type(2))) __bf16 bf16x2;
typedef __attribute__((ext_vector_type(4))) short s16x4;
typedef __attribute__((ext_vector_type(4))) float f32x4;
typedef __attribute__((ext_vector_type(2))) float f32x2;
typedef __attribute__((ext_vector_type(4))) unsigned u32x4;
typedef __attribute__((ext_vector_type(2))) unsigned u32x2;
#define LAS __attribute__((address_space(3)))

#define KV_NT 1
#define NT_STR " nt"

constexpr int SQ = 48, DM = 512, NH = 32, SK = 1024, DH = 128, HD = NH * DH;
constexpr int NBLK = SK / 32;
constexpr int BLK_BYTES = 16384;
constexpr int LDS_ATTN = 10 * BLK_BYTES;
constexpr int QOFF = 6 * BLK_BYTES;
constexpr int XOFF1 = 7 * BLK_BYTES, XOFF2 = 6 * BLK_BYTES;
#define FILL_STEP 13
#define QSTAMP(n)
#define FILL1_LATE 0
#define NO_WQ_PF 0
#define WO_PF_IT 32
#define WO_GRAN 128
#define WO_PER 64
#define X_NT 0
#define WQ_AUX 0
#define WQPF_AUX 0
#define WS 0
#define WOI 31
#define O_AUX 1
#define WOAUX 0
#define W0 0
#define WPER 2
#define NRB 2
#define RB0 8
#define RBS 10
#define KPH 0
#define WO_MATCH 1
#define WQ_PF3 2
constexpr float SCL = 0.088388347648318447f * 1.4426950408889634f;
constexpr float RESCALE_THR = 6.0f;

__device__ __forceinline__ unsigned pack2(float lo, float hi) {
  f32x2 f = {lo, hi};
  return __builtin_bit_cast(unsigned, __builtin_convertvector(f, bf16x2));
}
__device__ __forceinline__ f32x4 mfma16(u32x4 a, u32x4 b, f32x4 c) {
  return __builtin_amdgcn_mfma_f32_16x16x32_bf16(__builtin_bit_cast(bf16x8, a), __builtin_bit_cast(bf16x8, b), c, 0, 0, 0);
}
__device__ __forceinline__ float xg_max(float v) {
  auto r = __builtin_amdgcn_permlane16_swap(__float_as_uint(v), __float_as_uint(v), false, false);
  v = fmaxf(__uint_as_float(r[0]), __uint_as_float(r[1]));
  auto s = __builtin_amdgcn_permlane32_swap(__float_as_uint(v), __float_as_uint(v), false, false);
  return fmaxf(__uint_as_float(s[0]), __uint_as_float(s[1]));
}
__device__ __forceinline__ float max3(float a, float b, float c) {
  return __builtin_elementwise_maximum(__builtin_elementwise_maximum(a, b), c);
}
#define SB() __builtin_amdgcn_sched_barrier(0)

#define DMA4_BODY(POL)                                                        \
  unsigned keep;                                                              \
  asm volatile(                                                               \
      "s_nop 4\n\ts_mov_b32 %0, m0\n\ts_mov_b32 m0, %1\n\ts_nop 0\n\t"      \
      "global_load_lds_dwordx4 %3, %2" POL "\n\t"                             \
      "global_load_lds_dwordx4 %4, %2 offset:1024" POL "\n\t"                 \
      "global_load_lds_dwordx4 %5, %2 offset:2048" POL "\n\t"                 \
      "global_load_lds_dwordx4 %6, %2 offset:3072" POL "\n\t"                 \
      "s_mov_b32 m0, %0"                                                      \
      : "=&s"(keep)                                                           \
      : "s"(m), "s"(g), "v"(o0), "v"(o1), "v"(o2), "v"(o3)                    \
      : "memory")
__device__ __forceinline__ void dma4(const char* g, unsigned m, unsigned o0, unsigned o1, unsigned o2, unsigned o3) { DMA4_BODY(NT_STR); }
__device__ __forceinline__ void dma4c(const char* g, unsigned m, unsigned o0, unsigned o1, unsigned o2, unsigned o3) {
  DMA4_BODY("");
}
#define WAIT_VM(n) asm volatile("s_waitcnt vmcnt(" #n ")" ::: "memory")
#define WAIT_LGKM0() asm volatile("s_waitcnt lgkmcnt(0)" ::: "memory")
#define BAR() asm volatile("s_barrier" ::: "memory")

typedef __amdgpu_buffer_rsrc_t rsrc_t;
__device__ __forceinline__ rsrc_t mkrs(const void* p, int bytes) { return __builtin_amdgcn_make_buffer_rsrc((void*)p, (short)0, bytes, 0x00020000); }
__device__ __forceinline__ u32x4 bld128(rsrc_t rs, unsigned vo, unsigned so) { return __builtin_amdgcn_raw_buffer_load_b128(rs, vo, so, 0); }
__device__ __forceinline__ u32x2 bld64(rsrc_t rs, unsigned vo, unsigned so) { return __builtin_amdgcn_raw_buffer_load_b64(rs, vo, so, 0); }

struct AttnArgs {
  const char* x[2];
  const char* k[2];
  const char* v[2];
  const char* wq[2];
  const char* wo[2];
  char* o;
};

template <bool XLDS>
__device__ __forceinline__ void qproj(u32x4 (&qf)[3][4], const char* wq, int wbytes, const char* x, char* smem, unsigned xoff, int lane) {
  f32x4 acc[3][8];
#pragma unroll
  for (int t = 0; t < 3; ++t)
#pragma unroll
    for (int e = 0; e < 8; ++e) acc[t][e] = f32x4{0.f, 0.f, 0.f, 0.f};
  const rsrc_t rw = mkrs(wq, wbytes);
  const rsrc_t rx = mkrs(x, SQ * DM * 2);
  const unsigned g = lane >> 4, r = lane & 15;
  const unsigned wv = g * (32 * HD * 2) + r * 16;
  const unsigned xv = r * (DM * 2) + g * 64;
  const unsigned xl = xoff + r * (DM * 2) + 16 * ((4 * g) ^ (((r & 3) << 2) | (r >> 2)));
  constexpr int XA = XLDS ? 1 : 2;
  u32x4 wb[3][8], xb[XA + 1][3];
  auto loadw = [&](int s, int jj) { wb[s % 3][jj] = bld128(rw, wv, (128 * (s >> 2) + 8 * (s & 3) + jj) * (HD * 2)); };
  auto loadx = [&](int s, int t) {
    if (XLDS) xb[s % (XA + 1)][t] = *(const LAS u32x4*)((LAS char*)smem + (xl ^ (16 * (s & 3))) + t * 16 * DM * 2 + (s >> 2) * 256);
    else xb[s % (XA + 1)][t] = bld128(rx, xv + (s >> 2) * 256 + (s & 3) * 16, t * 16 * DM * 2);
  };
  u32x4 f[2];
  auto tr = [&](int n, int t) {
    const int s = n >> 3, e = n & 7;
    f[n & 1][t] = __builtin_amdgcn_perm(wb[s % 3][2 * t + 1][e >> 1], wb[s % 3][2 * t][e >> 1], (e & 1) ? 0x07060302u : 0x05040100u);
  };
#pragma unroll
  for (int s = 0; s < 2; ++s) {
#pragma unroll
    for (int jj = 0; jj < 8; ++jj) loadw(s, jj);
    if (!XLDS)
#pragma unroll
      for (int t = 0; t < 3; ++t) loadx(s, t);
  }
  BAR();
  QSTAMP(0);
  if (XLDS)
#pragma unroll
    for (int t = 0; t < 3; ++t) loadx(0, t);
#pragma unroll
  for (int t = 0; t < 4; ++t) tr(0, t);
#pragma unroll
  for (int s = 0; s < 16; ++s)
#pragma unroll
  for (int e = 0; e < 8; ++e) {
    const int n = 8 * s + e;
    if (n == 8 * FILL_STEP + 4) BAR();
    SB();
    acc[0][e] = mfma16(f[n & 1], xb[s % (XA + 1)][0], acc[0][e]);
    if (n + 1 < 128) { tr(n + 1, 0); tr(n + 1, 1); }
    SB();
    acc[1][e] = mfma16(f[n & 1], xb[s % (XA + 1)][1], acc[1][e]);
    if (n + 1 < 128) { tr(n + 1, 2); tr(n + 1, 3); }
    SB();
    acc[2][e] = mfma16(f[n & 1], xb[s % (XA + 1)][2], acc[2][e]);
    if (e < 4 && s + 2 < 16) { loadw(s + 2, 2 * e); loadw(s + 2, 2 * e + 1); }
    if (e >= 4 && e < 7 && s + XA < 16) loadx(s + XA, e - 4);
    if (n == 7) QSTAMP(1);
    if (n == 63) QSTAMP(2);
  }
  SB();
#pragma unroll
  for (int t = 0; t < 3; ++t)
#pragma unroll
    for (int s = 0; s < 4; ++s)
#pragma unroll
      for (int e = 0; e < 4; ++e) qf[t][s][e] = pack2(acc[t][2 * e][s], acc[t][2 * e + 1][s]);
}

__device__ __forceinline__ void store_tile(char* op, const f32x4 (&o)[8], const f32x4& l) {
  const float inv = 1.0f / l[0];
#pragma unroll
  for (int m = 0; m < 4; ++m) {
    u32x4 f;
    f[0] = pack2(o[2 * m][0] * inv, o[2 * m][1] * inv);
    f[1] = pack2(o[2 * m][2] * inv, o[2 * m][3] * inv);
    f[2] = pack2(o[2 * m + 1][0] * inv, o[2 * m + 1][1] * inv);
    f[3] = pack2(o[2 * m + 1][2] * inv, o[2 * m + 1][3] * inv);
    asm volatile("global_store_dwordx4 %0, %1, off sc0 sc1\n\ts_nop 2" ::"v"(op + m * 1024), "v"(f) : "memory");
  }
}

__global__ __launch_bounds__(512, 2) void attn_kernel(AttnArgs a) {
  extern __shared__ __attribute__((aligned(16))) char smem[];
  const int lane = threadIdx.x & 63;
  const int w = __builtin_amdgcn_readfirstlane(threadIdx.x >> 6);
  const int xcd = blockIdx.x & 7, j = blockIdx.x >> 3;
  const bool type1 = j < 16;
  const int b = j & 15;
  const unsigned lds0 = (unsigned)(uintptr_t)(LAS char*)smem;
  const int nst = type1 ? 2 : 3, rdepth = type1 ? 5 : 3;
  const unsigned xoff = type1 ? XOFF1 : XOFF2;

  auto warm_wo = [&](int per, int nbar) {
    const rsrc_t rw = mkrs(a.wo[xcd & 1], HD * DM * 2);
    const unsigned vo = (unsigned)((256 * b + (lane >> 2)) * (DM * 2) + (xcd >> 2) * 512 + (lane & 3) * 128);
    unsigned t[16];
#pragma unroll
    for (int q = 0; q < 16; ++q) {
      if (q % per == 0) BAR();
      t[q] = __builtin_amdgcn_raw_buffer_load_b32(rw, vo, q * 16 * (DM * 2), WOAUX);
    }
    for (int i = 0; i < nbar; ++i) BAR();
#pragma unroll
    for (int q = 0; q < 16; ++q) asm volatile("" ::"v"(t[q]));
  };
  if (w >= (type1 ? 5 : 4)) {
    const unsigned g = lane >> 4, r = lane & 15;
    unsigned tq[8];
#pragma unroll
    for (int q = 0; q < 8; ++q) tq[q] = 0;
    if (type1 && w == 5) {
      const unsigned vo = (unsigned)((32 * b + (lane >> 3)) * (HD * 2) + xcd * 1024 + (lane & 7) * 128);
      const unsigned v1 = vo + 8u * HD * 2, v2 = vo + 16u * HD * 2, v3 = vo + 24u * HD * 2;
      asm volatile(
          "s_nop 4\n\t"
          "global_load_dword %0, %8, %13\n\tglobal_load_dword %1, %9, %13\n\tglobal_load_dword %2, %10, %13\n\tglobal_load_dword %3, %11, %13\n\t"
          "global_load_dword %4, %8, %12\n\tglobal_load_dword %5, %9, %12\n\tglobal_load_dword %6, %10, %12\n\tglobal_load_dword %7, %11, %12"
          : "=&v"(tq[0]), "=&v"(tq[1]), "=&v"(tq[2]), "=&v"(tq[3]), "=&v"(tq[4]), "=&v"(tq[5]), "=&v"(tq[6]), "=&v"(tq[7])
          : "v"(vo), "v"(v1), "v"(v2), "v"(v3), "s"(a.wq[0]), "s"(a.wq[1])
          : "memory");
    }
    const int li = 7 - w;
    {
      const char* xg = a.x[type1 ? 0 : 1] + (size_t)b * (SQ * DM * 2);
      const int nq = type1 ? 4 : 3;
      for (int q = 0; q < nq; ++q) {
        const int rg = li * nq + q;
        const unsigned base = g * 256 + 16 * (r ^ (unsigned)(rg & 3));
        dma4c(xg + rg * 4096, lds0 + xoff + rg * 4096, base, base ^ 64u, base ^ 128u, base ^ 192u);
      }
    }
    const int nu = (type1 && li == 2) ? 2 : 3;
    const char* up[3];
    unsigned ul[3], uo[3][4];
    bool uA[3];
#pragma unroll
    for (int u = 0; u < 3; ++u) {
      const int id = (li * 3 + u) & (type1 ? 7 : 15);
      const int st = id >> 2, isv = (id >> 1) & 1, hf = id & 1;
      uA[u] = type1 && st == 1;
      const size_t sidx = uA[u] ? (size_t)(b * 8 + xcd) : (size_t)(b * 32 + 4 * xcd + (type1 ? 0 : 1 + st));
      const char* kb = uA[u] ? a.k[0] : a.k[1];
      const char* vb = uA[u] ? a.v[0] : a.v[1];
      up[u] = (isv ? vb : kb) + sidx * (SK * DH * 2) + hf * 4096;
      ul[u] = lds0 + st * BLK_BYTES + isv * 8192 + hf * 4096;
#pragma unroll
      for (int q = 0; q < 4; ++q) uo[u][q] = g * 256 + 16 * (r ^ (isv ? (((q & 1) * 4 + g) << 1) : ((g << 2) | q)));
    }
    const unsigned sstride = nst * BLK_BYTES;
    auto issue = [&](int blk, int slot, int which) {
#pragma unroll
      for (int u = 0; u < 3; ++u)
        if (u < nu && (which == 0 || (which == 2) == uA[u])) dma4(up[u] + blk * 8192, ul[u] + slot * sstride, uo[u][0], uo[u][1], uo[u][2], uo[u][3]);
    };
    int slot = rdepth - 1;
    if (type1) {
      issue(0, 0, 0); issue(1, 1, 0); issue(2, 2, 0);
      u32x4 rb[NRB][3][4];
#pragma unroll
      for (int u = 0; u < 3; ++u) {
        const rsrc_t rs = mkrs(up[u < nu ? u : 0], SK * DH * 2 - 4096);
#pragma unroll
        for (int k = 0; k < NRB; ++k)
#pragma unroll
          for (int q = 0; q < 4; ++q)
            rb[k][u][q] = (u < nu) ? __builtin_amdgcn_raw_buffer_load_b128(rs, uo[u][q] + q * 1024, (RB0 + k * RBS) * 8192, 2) : u32x4{0, 0, 0, 0};
      }
      asm volatile("s_waitcnt vmcnt(%0)" ::"n"(8 * NRB) : "memory");
      BAR();
      BAR();
      WAIT_VM(0);
#pragma unroll
      for (int k = 0; k < NRB; ++k)
#pragma unroll
        for (int u = 0; u < 3; ++u) asm volatile("" : "+v"(rb[k][u][0]), "+v"(rb[k][u][1]), "+v"(rb[k][u][2]), "+v"(rb[k][u][3]));
      auto isreg = [&](int blk) { return blk >= RB0 && blk < RB0 + NRB * RBS && (blk - RB0) % RBS == 0; };
      u32x4 q1[4];
      f32x4 o1[8], ls = f32x4{0.f, 0.f, 0.f, 0.f};
#pragma unroll
      for (int c = 0; c < 8; ++c) o1[c] = f32x4{0.f, 0.f, 0.f, 0.f};
      float mr = -__builtin_inff(), th = -__builtin_inff();
      u32x4 ones = {0x3f803f80u, 0x3f803f80u, 0x3f803f80u, 0x3f803f80u};
      asm volatile("" : "+v"(ones));
      const unsigned fk = ((r & 3) << 2) | (r >> 2), vrow = 4 * g + (r >> 2);
      const unsigned vra = 8192 + vrow * 256 + 8 * (r & 1), vsw = (vrow & 7) << 1, vp = (r >> 1) & 1;
      int cs = 0;
      for (int i = 0; i < NBLK; ++i) {
        int cnt = (i == 3) ? -1 : 0;
#pragma unroll
        for (int d = 1; d <= 3; ++d) cnt += (i + d < NBLK && !isreg(i + d)) ? 1 : 0;
        if (nu == 3) { if (cnt == 3) WAIT_VM(36); else if (cnt == 2) WAIT_VM(24); else if (cnt == 1) WAIT_VM(12); else WAIT_VM(0); }
        else { if (cnt == 3) WAIT_VM(24); else if (cnt == 2) WAIT_VM(16); else if (cnt == 1) WAIT_VM(8); else WAIT_VM(0); }
        BAR();
        if (i == 0) {
#pragma unroll
          for (int c = 0; c < 4; ++c) q1[c] = *(const LAS u32x4*)((LAS char*)smem + QOFF + (li * 4 + c) * 1024 + lane * 16);
          issue(3, 3, 2);
        }
        if (i == 1) issue(3, 3, 1);
        if (isreg(i + 4)) {
#pragma unroll
          for (int k = 0; k < NRB; ++k)
            if (i + 4 == RB0 + k * RBS) {
#pragma unroll
              for (int u = 0; u < 3; ++u)
                if (u < nu)
#pragma unroll
                  for (int q = 0; q < 4; ++q)
                    *(LAS u32x4*)(uintptr_t)(ul[u] + slot * sstride + q * 1024 + lane * 16) = rb[k][u][q];
            }
          WAIT_LGKM0();
        } else if (i + 4 < NBLK) {
          issue(i + 4, slot, 0);
        }
        slot = (slot + 1 == rdepth) ? 0 : slot + 1;
        const unsigned sb = cs * sstride;
        f32x4 sc[2] = {f32x4{0.f, 0.f, 0.f, 0.f}, f32x4{0.f, 0.f, 0.f, 0.f}};
#pragma unroll
        for (int c = 0; c < 8; ++c) {
          const u32x4 kf = *(const LAS u32x4*)((LAS char*)smem + sb + r * 256 + 16 * ((4 * g + (c & 3)) ^ fk) + (c >> 2) * 4096);
          sc[c >> 2] = mfma16(kf, q1[c & 3], sc[c >> 2]);
        }
        u32x4 vf[8], pf;
#pragma unroll
        for (int c = 0; c < 8; ++c) {
          const unsigned ad = sb + vra + 16 * ((2 * c + vp) ^ vsw);
          s16x4 l0 = __builtin_amdgcn_ds_read_tr16_b64_v4i16((LAS s16x4*)((LAS char*)smem + ad));
          s16x4 l1 = __builtin_amdgcn_ds_read_tr16_b64_v4i16((LAS s16x4*)((LAS char*)smem + ad + 4096));
          u32x2 u0 = __builtin_bit_cast(u32x2, l0), u1 = __builtin_bit_cast(u32x2, l1);
          vf[c] = u32x4{u0[0], u0[1], u1[0], u1[1]};
        }
        float lm = max3(max3(sc[0][0], sc[0][1], sc[0][2]), sc[0][3], sc[1][0]);
        lm = __builtin_elementwise_maximum(max3(lm, sc[1][1], sc[1][2]), sc[1][3]);
        if (__builtin_expect(__any(!(lm <= th)), 0)) {
          const float mn = fmaxf(mr, xg_max(lm) * SCL);
          const float al = __builtin_amdgcn_exp2f(mr - mn);
          mr = mn;
          th = (mn + RESCALE_THR) * (1.0f / SCL);
          ls *= al;
#pragma unroll
          for (int c = 0; c < 8; ++c) o1[c] *= al;
        }
#pragma unroll
        for (int e = 0; e < 4; ++e)
          pf[e] = pack2(__builtin_amdgcn_exp2f(fmaf(sc[e >> 1][2 * (e & 1)], SCL, -mr)), __builtin_amdgcn_exp2f(fmaf(sc[e >> 1][2 * (e & 1) + 1], SCL, -mr)));
#pragma unroll
        for (int c = 0; c < 8; ++c) o1[c] = mfma16(vf[c], pf, o1[c]);
        ls = mfma16(ones, pf, ls);
        cs = (cs + 1 == rdepth) ? 0 : cs + 1;
        WAIT_LGKM0();
      }
      store_tile(a.o + ((size_t)(512 + b * 32 + 4 * xcd) * 12 + li * 4) * 1024 + lane * 16, o1, ls);
    } else {
      WAIT_VM(0);
      BAR();
      BAR();
      issue(0, 0, 0); issue(1, 1, 0);
      for (int i = 0; i < NBLK; ++i) {
        if (i + 1 < NBLK) WAIT_VM(12); else WAIT_VM(0);
        BAR();
        if (i + 2 < NBLK) issue(i + 2, slot, 0);
        slot = (slot + 1 == rdepth) ? 0 : slot + 1;
      }
    }
#pragma unroll
    for (int q = 0; q < 8; ++q) asm volatile("" ::"v"(tq[q]));
    return;
  }
  if (!type1 && w == 3) {
    if (W0) return;
    for (int i = 0; i < NBLK + 1; ++i) BAR();
    warm_wo(16, 0);
    return;
  }

  const bool roleA = type1 && w < 4;
  const int p = roleA ? 0 : 1;
  const int h = roleA ? 4 * xcd + w : (type1 ? 4 * xcd : 4 * xcd + 1 + w);
  const unsigned ringoff = (unsigned)((type1 ? (roleA ? 1 : 0) : w) * BLK_BYTES);
  const bool xlds = !(type1 && w == 4);

  u32x4 qf[3][4];
  if (xlds) qproj<true>(qf, a.wq[p] + h * 256, DM * HD * 2 - h * 256, a.x[p] + b * (SQ * DM * 2), smem, xoff, lane);
  else qproj<false>(qf, a.wq[p] + h * 256, DM * HD * 2 - h * 256, a.x[p] + b * (SQ * DM * 2), smem, xoff, lane);
  if (!xlds) {
#pragma unroll
    for (int c = 0; c < 12; ++c) *(LAS u32x4*)((LAS char*)smem + QOFF + c * 1024 + lane * 16) = qf[c >> 2][c & 3];
    WAIT_LGKM0();
    BAR();
    if (W0) {
      for (int i = 1; i < W0; ++i) BAR();
      warm_wo(WPER, NBLK - W0 - 16 / WPER);
    }
    return;
  }

  int lane2 = lane;
  asm volatile("" : "+v"(lane2));
  const int g = lane2 >> 4, r = lane2 & 15;
  f32x4 oacc[3][8];
#pragma unroll
  for (int t = 0; t < 3; ++t)
#pragma unroll
    for (int c = 0; c < 8; ++c) oacc[t][c] = f32x4{0.f, 0.f, 0.f, 0.f};
  float mrow[3] = {-__builtin_inff(), -__builtin_inff(), -__builtin_inff()};
  float thr[3] = {-__builtin_inff(), -__builtin_inff(), -__builtin_inff()};
  f32x4 lsum[3] = {f32x4{0.f, 0.f, 0.f, 0.f}, f32x4{0.f, 0.f, 0.f, 0.f}, f32x4{0.f, 0.f, 0.f, 0.f}};
  u32x4 ones = {0x3f803f80u, 0x3f803f80u, 0x3f803f80u, 0x3f803f80u};
  asm volatile("" : "+v"(ones));
  const unsigned fk = ((r & 3) << 2) | (r >> 2);
  unsigned kra[4];
#pragma unroll
  for (int s = 0; s < 4; ++s) kra[s] = r * 256 + 16 * ((4 * g + s) ^ fk);
  const unsigned vrow = 4 * g + (r >> 2);
  const unsigned vra = 8192 + vrow * 256 + 8 * (r & 1);
  const unsigned vsw = (vrow & 7) << 1, vp = (r >> 1) & 1;

  int slot = 0;
  for (int i = 0; i < NBLK; ++i) {
    const unsigned sb = ringoff + slot * nst * BLK_BYTES;
    WAIT_LGKM0();
    BAR();
    f32x4 sc[3][2];
    u32x4 kf[8], vf[8], pf[3];
    float lm = 0.f, pv[8];
#pragma unroll
    for (int s = 0; s < 8; ++s) kf[s] = *(const LAS u32x4*)((LAS char*)smem + sb + kra[s & 3] + (s >> 2) * 4096);
    auto qk = [&](int t, int s) {
      sc[t][s >> 2] = mfma16(kf[s], qf[t][s & 3], (s & 3) ? sc[t][s >> 2] : f32x4{0.f, 0.f, 0.f, 0.f});
    };
    auto ldv = [&](int c) {
      const unsigned ad = sb + vra + 16 * ((2 * c + vp) ^ vsw);
      s16x4 l0 = __builtin_amdgcn_ds_read_tr16_b64_v4i16((LAS s16x4*)((LAS char*)smem + ad));
      s16x4 l1 = __builtin_amdgcn_ds_read_tr16_b64_v4i16((LAS s16x4*)((LAS char*)smem + ad + 4096));
      u32x2 u0 = __builtin_bit_cast(u32x2, l0), u1 = __builtin_bit_cast(u32x2, l1);
      vf[c] = u32x4{u0[0], u0[1], u1[0], u1[1]};
    };
    auto mx = [&](int t, int q) {
      if (q == 0) lm = max3(sc[t][0][0], sc[t][0][1], sc[t][0][2]);
      else if (q == 1) lm = max3(lm, sc[t][0][3], sc[t][1][0]);
      else if (q == 2) lm = max3(lm, sc[t][1][1], sc[t][1][2]);
      else lm = __builtin_elementwise_maximum(lm, sc[t][1][3]);
    };
    auto rescale = [&](int t) {
      if (__builtin_expect(__any(!(lm <= thr[t])), 0)) {
        const float mn = fmaxf(mrow[t], xg_max(lm) * SCL);
        const float al = __builtin_amdgcn_exp2f(mrow[t] - mn);
        mrow[t] = mn;
        thr[t] = (mn + RESCALE_THR) * (1.0f / SCL);
        lsum[t] *= al;
#pragma unroll
        for (int c = 0; c < 8; ++c) oacc[t][c] *= al;
      }
    };
    auto ex = [&](int t, int e) {
      pv[e] = __builtin_amdgcn_exp2f(fmaf(sc[t][e >> 2][e & 3], SCL, -mrow[t]));
      if (e & 1) {
        unsigned u = pack2(pv[e - 1], pv[e]);
        asm volatile("" : "+v"(u));
        pf[t][e >> 1] = u;
      } else {
        asm volatile("" : "+v"(pv[e]));
      }
    };
    auto pvm = [&](int t, int c) {
      if (c < 8) oacc[t][c] = mfma16(vf[c], pf[t], oacc[t][c]);
      else lsum[t] = mfma16(ones, pf[t], lsum[t]);
    };
    SB();
#pragma unroll
    for (int s = 0; s < 8; ++s) qk(0, s);
    SB();
#pragma unroll
    for (int s = 0; s < 8; ++s) {
      qk(1, s);
      if (s >= 2 && s < 6) mx(0, s - 2);
      SB();
    }
    rescale(0);
#pragma unroll
    for (int s = 0; s < 8; ++s) {
      qk(2, s);
      ldv(s);
      if (s & 1) ex(0, s >> 1);
      SB();
    }
#pragma unroll
    for (int s = 0; s < 4; ++s) { ex(0, 4 + s); mx(1, s); SB(); }
    rescale(1);
#pragma unroll
    for (int s = 0; s < 9; ++s) {
      pvm(0, s);
      if (s < 8) ex(1, s);
      if (s >= 5) mx(2, s - 5);
      SB();
    }
    rescale(2);
#pragma unroll
    for (int s = 0; s < 9; ++s) {
      pvm(1, s);
      if (s < 8) ex(2, s);
      SB();
    }
#pragma unroll
    for (int s = 0; s < 9; ++s) pvm(2, s);
    slot = (slot + 1 == rdepth) ? 0 : slot + 1;
  }

#pragma unroll
  for (int t = 0; t < 3; ++t) store_tile(a.o + ((size_t)(p * 512 + b * 32 + h) * 12 + t * 4) * 1024 + lane2 * 16, oacc[t], lsum[t]);
}

struct OutArgs {
  const char* wo[2];
  const char* o;
  char* out;
};
#define OP_PHASE 0
constexpr int OPD = 5;
constexpr int LDS_OPROJ = 8 * OPD * 4096;
__global__ __launch_bounds__(512, 2) void oproj_kernel(OutArgs a) {
  extern __shared__ __attribute__((aligned(16))) char smem[];
  const int lane = threadIdx.x & 63;
  const int w = __builtin_amdgcn_readfirstlane(threadIdx.x >> 6);
  const int xcd = blockIdx.x & 7, j = blockIdx.x >> 3;
  const int p = xcd & 1, b = ((xcd >> 1) & 1) * 8 + (j & 7), nb = (xcd >> 2) * 4 + (j >> 3);
  const int g = lane >> 4, r = lane & 15;
  const int ph = OP_PHASE == 1 ? (2 * (j & 7) + 4 * (j >> 3)) : OP_PHASE == 2 ? 4 * (j >> 3) : OP_PHASE == 3 ? 8 * (j >> 4) : 0;
  const char* wg = a.wo[p] + (size_t)w * 512 * (DM * 2) + nb * 128;
  const char* og = a.o + (size_t)(p * 512 + b * 32 + 4 * w) * 12288;
  const unsigned lw = (unsigned)(uintptr_t)(LAS char*)smem + w * (OPD * 4096);
  const unsigned so = (lane >> 3) * (DM * 2) + 16 * ((lane & 7) ^ (g << 1));
  const unsigned so1 = so + 7168, so2 = so + 2 * 7168, so3 = so + 3 * 7168, ov = lane * 16;
  const unsigned trow = 4 * g + (r >> 2);
  const unsigned tra = w * (OPD * 4096) + trow * 128 + 8 * (r & 1), tsw = ((trow >> 1) & 3) << 1, tp = (r >> 1) & 1;
  f32x4 acc[3][4];
#pragma unroll
  for (int t = 0; t < 3; ++t)
#pragma unroll
    for (int e = 0; e < 4; ++e) acc[t][e] = f32x4{0.f, 0.f, 0.f, 0.f};
  u32x4 ob[OPD][3];
  auto load = [&](int s) {
    const int q = (s + ph) & 15;
    dma4c(wg + q * (32 * DM * 2), lw + (s % OPD) * 4096, so, so1, so2, so3);
    const char* os = og + (q >> 2) * 12288 + (q & 3) * 1024 + 4096;
    asm volatile(
        "global_load_dwordx4 %0, %3, %4 offset:-4096\n\tglobal_load_dwordx4 %1, %3, %4\n\tglobal_load_dwordx4 %2, %3, %5"
        : "=&v"(ob[s % OPD][0]), "=&v"(ob[s % OPD][1]), "=&v"(ob[s % OPD][2])
        : "v"(ov), "s"(os), "s"(os + 4096)
        : "memory");
  };
#pragma unroll
  for (int s = 0; s < OPD; ++s) load(s);
#pragma unroll
  for (int s = 0; s < 16; ++s) {
    const int left = (15 - s < OPD - 1) ? 15 - s : OPD - 1;
    asm volatile("s_waitcnt vmcnt(%3)" : "+v"(ob[s % OPD][0]), "+v"(ob[s % OPD][1]), "+v"(ob[s % OPD][2]) : "n"(7 * left) : "memory");
    u32x4 f[4];
#pragma unroll
    for (int e = 0; e < 4; ++e) {
      const unsigned ad = tra + (s % OPD) * 4096 + 16 * ((2 * e + tp) ^ tsw);
      s16x4 l0 = __builtin_amdgcn_ds_read_tr16_b64_v4i16((LAS s16x4*)((LAS char*)smem + ad));
      s16x4 l1 = __builtin_amdgcn_ds_read_tr16_b64_v4i16((LAS s16x4*)((LAS char*)smem + ad + 2048));
      u32x2 u0 = __builtin_bit_cast(u32x2, l0), u1 = __builtin_bit_cast(u32x2, l1);
      f[e] = u32x4{u0[0], u0[1], u1[0], u1[1]};
    }
#pragma unroll
    for (int e = 0; e < 4; ++e)
#pragma unroll
      for (int t = 0; t < 3; ++t) acc[t][e] = mfma16(f[e], ob[s % OPD][t], acc[t][e]);
    if (s + OPD < 16) {
      asm volatile("s_waitcnt lgkmcnt(0)" : "+v"(acc[2][3]) : : "memory");
      load(s + OPD);
    }
  }
  f32x4* red = (f32x4*)smem;
  constexpr int RS = OPD * 4096 / 16;
#pragma unroll
  for (int t = 0; t < 3; ++t)
#pragma unroll
    for (int e = 0; e < 4; ++e) red[w * RS + (t * 4 + e) * 64 + (((lane & 15) << 2) | (lane >> 4))] = acc[t][e];
  __syncthreads();
  if (w < 6) {
    const int t = w >> 1, e = (w & 1) * 2 + (lane & 2 ? 1 : 0), q = lane >> 2, gh = lane & 1;
    f32x4 s0 = f32x4{0.f, 0.f, 0.f, 0.f}, s1 = s0;
#pragma unroll
    for (int ww = 0; ww < 8; ++ww) {
      s0 += red[ww * RS + (t * 4 + e) * 64 + q * 4 + 2 * gh];
      s1 += red[ww * RS + (t * 4 + e) * 64 + q * 4 + 2 * gh + 1];
    }
    u32x4 f = {pack2(s0[0], s0[1]), pack2(s0[2], s0[3]), pack2(s1[0], s1[1]), pack2(s1[2], s1[3])};
    *(u32x4*)(a.out + ((size_t)(b * SQ + 16 * t + q) * 1024 + p * 512 + nb * 64 + 16 * e + 8 * gh) * 2) = f;
  }
}

extern "C" void kernel_launch(void* const* d_in, const int* in_sizes, int n_in, void* d_out, int out_size, void* d_ws,
                              size_t ws_size, hipStream_t stream) {
  (void)in_sizes; (void)n_in; (void)out_size; (void)ws_size;
  AttnArgs a;
  a.x[0] = (const char*)d_in[0]; a.k[0] = (const char*)d_in[1]; a.v[0] = (const char*)d_in[2]; a.wq[0] = (const char*)d_in[3];
  a.x[1] = (const char*)d_in[5]; a.k[1] = (const char*)d_in[6]; a.v[1] = (const char*)d_in[7]; a.wq[1] = (const char*)d_in[8];
  a.wo[0] = (const char*)d_in[4]; a.wo[1] = (const char*)d_in[9];
  a.o = (char*)d_ws;
  OutArgs oa;
  oa.wo[0] = (const char*)d_in[4]; oa.wo[1] = (const char*)d_in[9]; oa.o = (const char*)d_ws; oa.out = (char*)d_out;
  (void)hipFuncSetAttribute((const void*)attn_kernel, hipFuncAttributeMaxDynamicSharedMemorySize, LDS_ATTN);
  (void)hipFuncSetAttribute((const void*)oproj_kernel, hipFuncAttributeMaxDynamicSharedMemorySize, LDS_OPROJ);
  hipLaunchKernelGGL(attn_kernel, dim3(256), dim3(512), LDS_ATTN, stream, a);
  hipLaunchKernelGGL(oproj_kernel, dim3(256), dim3(512), LDS_OPROJ, stream, oa);
}
```

```cpp
#include <hip/hip_runtime.h>
#include <hip/hip_bf16.h>
#include <stdint.h>

typedef __attribute__((ext_vector_type(8))) __bf16 bf16x8;
typedef __attribute__((ext_vector_type(2))) __bf16 bf16x2;
typedef __attribute__((ext_vector_type(4))) short s16x4;
typedef __attribute__((ext_vector_type(4))) float f32x4;
typedef __attribute__((ext_vector_type(2))) float f32x2;
typedef __attribute__((ext_vector_type(4))) unsigned u32x4;
typedef __attribute__((ext_vector_type(2))) unsigned u32x2;
#define LAS __attribute__((address_space(3)))

constexpr int SQ = 48, DM = 512, NH = 32, SK = 1024, DH = 128, HD = NH * DH;
constexpr int NBLK = SK / 32;
constexpr int BLK_BYTES = 16384;
constexpr int LDS_ATTN = 10 * BLK_BYTES;
constexpr int QOFF = 6 * BLK_BYTES;
constexpr int XOFF1 = 7 * BLK_BYTES, XOFF2 = 6 * BLK_BYTES;
constexpr int FILL_STEP = 13;
constexpr int NRB = 2, RB0 = 8, RBS = 10;
constexpr float SCL = 0.088388347648318447f * 1.4426950408889634f;
constexpr float RESCALE_THR = 6.0f;

__device__ __forceinline__ unsigned pack2(float lo, float hi) {
  f32x2 f = {lo, hi};
  return __builtin_bit_cast(unsigned, __builtin_convertvector(f, bf16x2));
}
__device__ __forceinline__ f32x4 mfma16(u32x4 a, u32x4 b, f32x4 c) {
  return __builtin_amdgcn_mfma_f32_16x16x32_bf16(__builtin_bit_cast(bf16x8, a), __builtin_bit_cast(bf16x8, b), c, 0, 0, 0);
}
__device__ __forceinline__ float xg_max(float v) {
  auto r = __builtin_amdgcn_permlane16_swap(__float_as_uint(v), __float_as_uint(v), false, false);
  v = fmaxf(__uint_as_float(r[0]), __uint_as_float(r[1]));
  auto s = __builtin_amdgcn_permlane32_swap(__float_as_uint(v), __float_as_uint(v), false, false);
  return fmaxf(__uint_as_float(s[0]), __uint_as_float(s[1]));
}
__device__ __forceinline__ float max3(float a, float b, float c) {
  return __builtin_elementwise_maximum(__builtin_elementwise_maximum(a, b), c);
}
#define SB() __builtin_amdgcn_sched_barrier(0)

#define DMA4_BODY(POL)                                                        \
  unsigned keep;                                                              \
  asm volatile(                                                               \
      "s_nop 4\n\ts_mov_b32 %0, m0\n\ts_mov_b32 m0, %1\n\ts_nop 0\n\t"      \
      "global_load_lds_dwordx4 %3, %2" POL "\n\t"                             \
      "global_load_lds_dwordx4 %4, %2 offset:1024" POL "\n\t"                 \
      "global_load_lds_dwordx4 %5, %2 offset:2048" POL "\n\t"                 \
      "global_load_lds_dwordx4 %6, %2 offset:3072" POL "\n\t"                 \
      "s_mov_b32 m0, %0"                                                      \
      : "=&s"(keep)                                                           \
      : "s"(m), "s"(g), "v"(o0), "v"(o1), "v"(o2), "v"(o3)                    \
      : "memory")
__device__ __forceinline__ void dma4(const char* g, unsigned m, unsigned o0, unsigned o1, unsigned o2, unsigned o3) { DMA4_BODY(" nt"); }
__device__ __forceinline__ void dma4c(const char* g, unsigned m, unsigned o0, unsigned o1, unsigned o2, unsigned o3) { DMA4_BODY(""); }
#define WAIT_VM(n) asm volatile("s_waitcnt vmcnt(" #n ")" ::: "memory")
#define WAIT_LGKM0() asm volatile("s_waitcnt lgkmcnt(0)" ::: "memory")
#define BAR() asm volatile("s_barrier" ::: "memory")

typedef __amdgpu_buffer_rsrc_t rsrc_t;
__device__ __forceinline__ rsrc_t mkrs(const void* p, int bytes) { return __builtin_amdgcn_make_buffer_rsrc((void*)p, (short)0, bytes, 0x00020000); }
__device__ __forceinline__ u32x4 bld128(rsrc_t rs, unsigned vo, unsigned so) { return __builtin_amdgcn_raw_buffer_load_b128(rs, vo, so, 0); }

#define WARM_CODE(TAIL, OFF)                                                                     \
  ({                                                                                             \
    unsigned long long pc_;                                                                      \
    int len_;                                                                                    \
    asm volatile("s_getpc_b64 %0\n1:\n\ts_mov_b32 %1, " #TAIL "-1b" : "=s"(pc_), "=s"(len_));   \
    __builtin_amdgcn_raw_buffer_load_b32(mkrs((const void*)pc_, len_ > 0 ? len_ : 0), OFF, 0, 0); \
  })

struct AttnArgs {
  const char* x[2];
  const char* k[2];
  const char* v[2];
  const char* wq[2];
  const char* wo[2];
  char* o;
};

template <bool XLDS>
__device__ __forceinline__ void qproj(u32x4 (&qf)[3][4], const char* wq, int wbytes, const char* x, char* smem, unsigned xoff, int lane) {
  f32x4 acc[3][8];
#pragma unroll
  for (int t = 0; t < 3; ++t)
#pragma unroll
    for (int e = 0; e < 8; ++e) acc[t][e] = f32x4{0.f, 0.f, 0.f, 0.f};
  const rsrc_t rw = mkrs(wq, wbytes);
  const rsrc_t rx = mkrs(x, SQ * DM * 2);
  const unsigned g = lane >> 4, r = lane & 15;
  const unsigned wv = g * (32 * HD * 2) + r * 16;
  const unsigned xv = r * (DM * 2) + g * 64;
  const unsigned xl = xoff + r * (DM * 2) + 16 * ((4 * g) ^ (((r & 3) << 2) | (r >> 2)));
  constexpr int XA = XLDS ? 1 : 2;
  u32x4 wb[3][8], xb[XA + 1][3];
  auto loadw = [&](int s, int jj) { wb[s % 3][jj] = bld128(rw, wv, (128 * (s >> 2) + 8 * (s & 3) + jj) * (HD * 2)); };
  auto loadx = [&](int s, int t) {
    if (XLDS) xb[s % (XA + 1)][t] = *(const LAS u32x4*)((LAS char*)smem + (xl ^ (16 * (s & 3))) + t * 16 * DM * 2 + (s >> 2) * 256);
    else xb[s % (XA + 1)][t] = bld128(rx, xv + (s >> 2) * 256 + (s & 3) * 16, t * 16 * DM * 2);
  };
  u32x4 f[2];
  auto tr = [&](int n, int t) {
    const int s = n >> 3, e = n & 7;
    f[n & 1][t] = __builtin_amdgcn_perm(wb[s % 3][2 * t + 1][e >> 1], wb[s % 3][2 * t][e >> 1], (e & 1) ? 0x07060302u : 0x05040100u);
  };
  const bool late = false;
  if (late) BAR();
#pragma unroll
  for (int s = 0; s < 2; ++s) {
#pragma unroll
    for (int jj = 0; jj < 8; ++jj) loadw(s, jj);
    if (!XLDS)
#pragma unroll
      for (int t = 0; t < 3; ++t) loadx(s, t);
  }
  if (!late) BAR();
  if (XLDS)
#pragma unroll
    for (int t = 0; t < 3; ++t) loadx(0, t);
#pragma unroll
  for (int t = 0; t < 4; ++t) tr(0, t);
#pragma unroll
  for (int s = 0; s < 16; ++s)
#pragma unroll
  for (int e = 0; e < 8; ++e) {
    const int n = 8 * s + e;
    if (n == 8 * FILL_STEP + 4) BAR();
    SB();
    acc[0][e] = mfma16(f[n & 1], xb[s % (XA + 1)][0], acc[0][e]);
    if (n + 1 < 128) { tr(n + 1, 0); tr(n + 1, 1); }
    SB();
    acc[1][e] = mfma16(f[n & 1], xb[s % (XA + 1)][1], acc[1][e]);
    if (n + 1 < 128) { tr(n + 1, 2); tr(n + 1, 3); }
    SB();
    acc[2][e] = mfma16(f[n & 1], xb[s % (XA + 1)][2], acc[2][e]);
    if (e < 4 && s + 2 < 16) { loadw(s + 2, 2 * e); loadw(s + 2, 2 * e + 1); }
    if (e >= 4 && e < 7 && s + XA < 16) loadx(s + XA, e - 4);
  }
  SB();
#pragma unroll
  for (int t = 0; t < 3; ++t)
#pragma unroll
    for (int s = 0; s < 4; ++s)
#pragma unroll
      for (int e = 0; e < 4; ++e) qf[t][s][e] = pack2(acc[t][2 * e][s], acc[t][2 * e + 1][s]);
}

__device__ __forceinline__ void store_tile(char* op, const f32x4 (&o)[8], const f32x4& l) {
  const float inv = 1.0f / l[0];
#pragma unroll
  for (int m = 0; m < 4; ++m) {
    u32x4 f;
    f[0] = pack2(o[2 * m][0] * inv, o[2 * m][1] * inv);
    f[1] = pack2(o[2 * m][2] * inv, o[2 * m][3] * inv);
    f[2] = pack2(o[2 * m + 1][0] * inv, o[2 * m + 1][1] * inv);
    f[3] = pack2(o[2 * m + 1][2] * inv, o[2 * m + 1][3] * inv);
    asm volatile("global_store_dwordx4 %0, %1, off sc0 sc1\n\ts_nop 2" ::"v"(op + m * 1024), "v"(f) : "memory");
  }
}

__global__ __launch_bounds__(512, 2) void attn_kernel(AttnArgs a) {
  extern __shared__ __attribute__((aligned(16))) char smem[];
  const int lane = threadIdx.x & 63;
  const int w = __builtin_amdgcn_readfirstlane(threadIdx.x >> 6);
  const int xcd = blockIdx.x & 7, j = blockIdx.x >> 3;
  const bool type1 = j < 16;
  const int b = j & 15;
  const unsigned lds0 = (unsigned)(uintptr_t)(LAS char*)smem;
  const int nst = type1 ? 2 : 3, rdepth = type1 ? 5 : 3;
  const unsigned xoff = type1 ? XOFF1 : XOFF2;

  if (w >= (type1 ? 5 : 4)) {
    const unsigned g = lane >> 4, r = lane & 15;
    unsigned tq[8];
#pragma unroll
    for (int q = 0; q < 8; ++q) tq[q] = 0;
    if (type1 && w == 5) {
      const unsigned vo = (unsigned)((32 * b + (lane >> 3)) * (HD * 2) + xcd * 1024 + (lane & 7) * 128);
      const unsigned v1 = vo + 8u * HD * 2, v2 = vo + 16u * HD * 2, v3 = vo + 24u * HD * 2;
      asm volatile(
          "s_nop 4\n\t"
          "global_load_dword %0, %8, %13\n\tglobal_load_dword %1, %9, %13\n\tglobal_load_dword %2, %10, %13\n\tglobal_load_dword %3, %11, %13\n\t"
          "global_load_dword %4, %8, %12\n\tglobal_load_dword %5, %9, %12\n\tglobal_load_dword %6, %10, %12\n\tglobal_load_dword %7, %11, %12"
          : "=&v"(tq[0]), "=&v"(tq[1]), "=&v"(tq[2]), "=&v"(tq[3]), "=&v"(tq[4]), "=&v"(tq[5]), "=&v"(tq[6]), "=&v"(tq[7])
          : "v"(vo), "v"(v1), "v"(v2), "v"(v3), "s"(a.wq[0]), "s"(a.wq[1])
          : "memory");
    }
    const int li = 7 - w;
    {
      const char* xg = a.x[type1 ? 0 : 1] + (size_t)b * (SQ * DM * 2);
      const int nq = type1 ? 4 : 3;
      for (int q = 0; q < nq; ++q) {
        const int rg = li * nq + q;
        const unsigned base = g * 256 + 16 * (r ^ (unsigned)(rg & 3));
        dma4c(xg + rg * 4096, lds0 + xoff + rg * 4096, base, base ^ 64u, base ^ 128u, base ^ 192u);
      }
    }
    const int nu = (type1 && li == 2) ? 2 : 3;
    const char* up[3];
    unsigned ul[3], uo[3][4];
    bool uA[3];
#pragma unroll
    for (int u = 0; u < 3; ++u) {
      const int id = (li * 3 + u) & (type1 ? 7 : 15);
      const int st = id >> 2, isv = (id >> 1) & 1, hf = id & 1;
      uA[u] = type1 && st == 1;
      const size_t sidx = uA[u] ? (size_t)(b * 8 + xcd) : (size_t)(b * 32 + 4 * xcd + (type1 ? 0 : 1 + st));
      const char* kb = uA[u] ? a.k[0] : a.k[1];
      const char* vb = uA[u] ? a.v[0] : a.v[1];
      up[u] = (isv ? vb : kb) + sidx * (SK * DH * 2) + hf * 4096;
      ul[u] = lds0 + st * BLK_BYTES + isv * 8192 + hf * 4096;
#pragma unroll
      for (int q = 0; q < 4; ++q) uo[u][q] = g * 256 + 16 * (r ^ (isv ? (((q & 1) * 4 + g) << 1) : ((g << 2) | q)));
    }
    const unsigned sstride = nst * BLK_BYTES;
    auto issue = [&](int blk, int slot, int which) {
#pragma unroll
      for (int u = 0; u < 3; ++u)
        if (u < nu && (which == 0 || (which == 2) == uA[u])) dma4(up[u] + blk * 8192, ul[u] + slot * sstride, uo[u][0], uo[u][1], uo[u][2], uo[u][3]);
    };
    int slot = rdepth - 1;
    if (type1) {
      issue(0, 0, 0); issue(1, 1, 0); issue(2, 2, 0);
      u32x4 rb[NRB][3][4];
#pragma unroll
      for (int u = 0; u < 3; ++u) {
        const rsrc_t rs = mkrs(up[u < nu ? u : 0], SK * DH * 2 - 4096);
#pragma unroll
        for (int k = 0; k < NRB; ++k)
#pragma unroll
          for (int q = 0; q < 4; ++q)
            rb[k][u][q] = (u < nu) ? __builtin_amdgcn_raw_buffer_load_b128(rs, uo[u][q] + q * 1024, (RB0 + k * RBS) * 8192, 2) : u32x4{0, 0, 0, 0};
      }
      asm volatile("s_waitcnt vmcnt(%0)" ::"n"(8 * NRB) : "memory");
      BAR();
      BAR();
      WAIT_VM(0);
#pragma unroll
      for (int k = 0; k < NRB; ++k)
#pragma unroll
        for (int u = 0; u < 3; ++u) asm volatile("" : "+v"(rb[k][u][0]), "+v"(rb[k][u][1]), "+v"(rb[k][u][2]), "+v"(rb[k][u][3]));
      auto isreg = [&](int blk) { return blk >= RB0 && blk < RB0 + NRB * RBS && (blk - RB0) % RBS == 0; };
      u32x4 q1[4];
      f32x4 o1[8], ls = f32x4{0.f, 0.f, 0.f, 0.f};
#pragma unroll
      for (int c = 0; c < 8; ++c) o1[c] = f32x4{0.f, 0.f, 0.f, 0.f};
      float mr = -__builtin_inff(), th = -__builtin_inff();
      u32x4 ones = {0x3f803f80u, 0x3f803f80u, 0x3f803f80u, 0x3f803f80u};
      asm volatile("" : "+v"(ones));
      const unsigned fk = ((r & 3) << 2) | (r >> 2), vrow = 4 * g + (r >> 2);
      const unsigned vra = 8192 + vrow * 256 + 8 * (r & 1), vsw = (vrow & 7) << 1, vp = (r >> 1) & 1;
      int cs = 0;
      for (int i = 0; i < NBLK; ++i) {
        int cnt = (i == 3) ? -1 : 0;
#pragma unroll
        for (int d = 1; d <= 3; ++d) cnt += (i + d < NBLK && !isreg(i + d)) ? 1 : 0;
        if (nu == 3) { if (cnt == 3) WAIT_VM(36); else if (cnt == 2) WAIT_VM(24); else if (cnt == 1) WAIT_VM(12); else WAIT_VM(0); }
        else { if (cnt == 3) WAIT_VM(24); else if (cnt == 2) WAIT_VM(16); else if (cnt == 1) WAIT_VM(8); else WAIT_VM(0); }
        BAR();
        if (i == 0) {
#pragma unroll
          for (int c = 0; c < 4; ++c) q1[c] = *(const LAS u32x4*)((LAS char*)smem + QOFF + (li * 4 + c) * 1024 + lane * 16);
          issue(3, 3, 2);
        }
        if (i == 1) issue(3, 3, 1);
        if (isreg(i + 4)) {
#pragma unroll
          for (int k = 0; k < NRB; ++k)
            if (i + 4 == RB0 + k * RBS) {
#pragma unroll
              for (int u = 0; u < 3; ++u)
                if (u < nu)
#pragma unroll
                  for (int q = 0; q < 4; ++q)
                    *(LAS u32x4*)(uintptr_t)(ul[u] + slot * sstride + q * 1024 + lane * 16) = rb[k][u][q];
            }
          WAIT_LGKM0();
        } else if (i + 4 < NBLK) {
          issue(i + 4, slot, 0);
        }
        slot = (slot + 1 == rdepth) ? 0 : slot + 1;
        const unsigned sb = cs * sstride;
        f32x4 sc[2] = {f32x4{0.f, 0.f, 0.f, 0.f}, f32x4{0.f, 0.f, 0.f, 0.f}};
#pragma unroll
        for (int c = 0; c < 8; ++c) {
          const u32x4 kf = *(const LAS u32x4*)((LAS char*)smem + sb + r * 256 + 16 * ((4 * g + (c & 3)) ^ fk) + (c >> 2) * 4096);
          sc[c >> 2] = mfma16(kf, q1[c & 3], sc[c >> 2]);
        }
        u32x4 vf[8], pf;
#pragma unroll
        for (int c = 0; c < 8; ++c) {
          const unsigned ad = sb + vra + 16 * ((2 * c + vp) ^ vsw);
          s16x4 l0 = __builtin_amdgcn_ds_read_tr16_b64_v4i16((LAS s16x4*)((LAS char*)smem + ad));
          s16x4 l1 = __builtin_amdgcn_ds_read_tr16_b64_v4i16((LAS s16x4*)((LAS char*)smem + ad + 4096));
          u32x2 u0 = __builtin_bit_cast(u32x2, l0), u1 = __builtin_bit_cast(u32x2, l1);
          vf[c] = u32x4{u0[0], u0[1], u1[0], u1[1]};
        }
        float lm = max3(max3(sc[0][0], sc[0][1], sc[0][2]), sc[0][3], sc[1][0]);
        lm = __builtin_elementwise_maximum(max3(lm, sc[1][1], sc[1][2]), sc[1][3]);
        if (__builtin_expect(__any(!(lm <= th)), 0)) {
          const float mn = fmaxf(mr, xg_max(lm) * SCL);
          const float al = __builtin_amdgcn_exp2f(mr - mn);
          mr = mn;
          th = (mn + RESCALE_THR) * (1.0f / SCL);
          ls *= al;
#pragma unroll
          for (int c = 0; c < 8; ++c) o1[c] *= al;
        }
#pragma unroll
        for (int e = 0; e < 4; ++e)
          pf[e] = pack2(__builtin_amdgcn_exp2f(fmaf(sc[e >> 1][2 * (e & 1)], SCL, -mr)), __builtin_amdgcn_exp2f(fmaf(sc[e >> 1][2 * (e & 1) + 1], SCL, -mr)));
#pragma unroll
        for (int c = 0; c < 8; ++c) o1[c] = mfma16(vf[c], pf, o1[c]);
        ls = mfma16(ones, pf, ls);
        cs = (cs + 1 == rdepth) ? 0 : cs + 1;
        WAIT_LGKM0();
      }
      store_tile(a.o + ((size_t)(512 + b * 32 + 4 * xcd) * 12 + li * 4) * 1024 + lane * 16, o1, ls);
    } else {
      WAIT_VM(0);
      BAR();
      BAR();
      issue(0, 0, 0); issue(1, 1, 0);
      for (int i = 0; i < NBLK; ++i) {
        if (i + 1 < NBLK) WAIT_VM(12); else WAIT_VM(0);
        BAR();
        if (i + 2 < NBLK) issue(i + 2, slot, 0);
        slot = (slot + 1 == rdepth) ? 0 : slot + 1;
      }
    }
#pragma unroll
    for (int q = 0; q < 8; ++q) asm volatile("" ::"v"(tq[q]));
    return;
  }
  if (!type1 && w == 3) {
    const unsigned tc = WARM_CODE(attn_code_tail, (b * 64 + lane) * 128);
    for (int i = 0; i < NBLK + 2; ++i) BAR();
    asm volatile("" ::"v"(tc));
    const rsrc_t rw = mkrs(a.wo[xcd & 1], HD * DM * 2);
    const unsigned vo = (unsigned)((256 * b + (lane >> 2)) * (DM * 2) + (xcd >> 2) * 512 + (lane & 3) * 128);
    unsigned t[16];
#pragma unroll
    for (int q = 0; q < 16; ++q) t[q] = __builtin_amdgcn_raw_buffer_load_b32(rw, vo, q * 16 * (DM * 2), 0);
#pragma unroll
    for (int q = 0; q < 16; ++q) asm volatile("" ::"v"(t[q]));
    return;
  }

  const bool roleA = type1 && w < 4;
  const int p = roleA ? 0 : 1;
  const int h = roleA ? 4 * xcd + w : (type1 ? 4 * xcd : 4 * xcd + 1 + w);
  const unsigned ringoff = (unsigned)((type1 ? (roleA ? 1 : 0) : w) * BLK_BYTES);
  const bool xlds = !(type1 && w == 4);

  u32x4 qf[3][4];
  if (xlds) qproj<true>(qf, a.wq[p] + h * 256, DM * HD * 2 - h * 256, a.x[p] + b * (SQ * DM * 2), smem, xoff, lane);
  else qproj<false>(qf, a.wq[p] + h * 256, DM * HD * 2 - h * 256, a.x[p] + b * (SQ * DM * 2), smem, xoff, lane);
  if (!xlds) {
#pragma unroll
    for (int c = 0; c < 12; ++c) *(LAS u32x4*)((LAS char*)smem + QOFF + c * 1024 + lane * 16) = qf[c >> 2][c & 3];
    WAIT_LGKM0();
    BAR();
    return;
  }

  int lane2 = lane;
  asm volatile("" : "+v"(lane2));
  const int g = lane2 >> 4, r = lane2 & 15;
  f32x4 oacc[3][8];
#pragma unroll
  for (int t = 0; t < 3; ++t)
#pragma unroll
    for (int c = 0; c < 8; ++c) oacc[t][c] = f32x4{0.f, 0.f, 0.f, 0.f};
  float mrow[3] = {-__builtin_inff(), -__builtin_inff(), -__builtin_inff()};
  float thr[3] = {-__builtin_inff(), -__builtin_inff(), -__builtin_inff()};
  f32x4 lsum[3] = {f32x4{0.f, 0.f, 0.f, 0.f}, f32x4{0.f, 0.f, 0.f, 0.f}, f32x4{0.f, 0.f, 0.f, 0.f}};
  u32x4 ones = {0x3f803f80u, 0x3f803f80u, 0x3f803f80u, 0x3f803f80u};
  asm volatile("" : "+v"(ones));
  const unsigned fk = ((r & 3) << 2) | (r >> 2);
  unsigned kra[4];
#pragma unroll
  for (int s = 0; s < 4; ++s) kra[s] = r * 256 + 16 * ((4 * g + s) ^ fk);
  const unsigned vrow = 4 * g + (r >> 2);
  const unsigned vra = 8192 + vrow * 256 + 8 * (r & 1);
  const unsigned vsw = (vrow & 7) << 1, vp = (r >> 1) & 1;

  int slot = 0;
  for (int i = 0; i < NBLK; ++i) {
    const unsigned sb = ringoff + slot * nst * BLK_BYTES;
    WAIT_LGKM0();
    BAR();
    f32x4 sc[3][2];
    u32x4 kf[8], vf[8], pf[3];
    float lm = 0.f, pv[8];
#pragma unroll
    for (int s = 0; s < 8; ++s) kf[s] = *(const LAS u32x4*)((LAS char*)smem + sb + kra[s & 3] + (s >> 2) * 4096);
    auto qk = [&](int t, int s) {
      sc[t][s >> 2] = mfma16(kf[s], qf[t][s & 3], (s & 3) ? sc[t][s >> 2] : f32x4{0.f, 0.f, 0.f, 0.f});
    };
    auto ldv = [&](int c) {
      const unsigned ad = sb + vra + 16 * ((2 * c + vp) ^ vsw);
      s16x4 l0 = __builtin_amdgcn_ds_read_tr16_b64_v4i16((LAS s16x4*)((LAS char*)smem + ad));
      s16x4 l1 = __builtin_amdgcn_ds_read_tr16_b64_v4i16((LAS s16x4*)((LAS char*)smem + ad + 4096));
      u32x2 u0 = __builtin_bit_cast(u32x2, l0), u1 = __builtin_bit_cast(u32x2, l1);
      vf[c] = u32x4{u0[0], u0[1], u1[0], u1[1]};
    };
    auto mx = [&](int t, int q) {
      if (q == 0) lm = max3(sc[t][0][0], sc[t][0][1], sc[t][0][2]);
      else if (q == 1) lm = max3(lm, sc[t][0][3], sc[t][1][0]);
      else if (q == 2) lm = max3(lm, sc[t][1][1], sc[t][1][2]);
      else lm = __builtin_elementwise_maximum(lm, sc[t][1][3]);
    };
    auto rescale = [&](int t) {
      if (__builtin_expect(__any(!(lm <= thr[t])), 0)) {
        const float mn = fmaxf(mrow[t], xg_max(lm) * SCL);
        const float al = __builtin_amdgcn_exp2f(mrow[t] - mn);
        mrow[t] = mn;
        thr[t] = (mn + RESCALE_THR) * (1.0f / SCL);
        lsum[t] *= al;
#pragma unroll
        for (int c = 0; c < 8; ++c) oacc[t][c] *= al;
      }
    };
    auto ex = [&](int t, int e) {
      pv[e] = __builtin_amdgcn_exp2f(fmaf(sc[t][e >> 2][e & 3], SCL, -mrow[t]));
      if (e & 1) {
        unsigned u = pack2(pv[e - 1], pv[e]);
        asm volatile("" : "+v"(u));
        pf[t][e >> 1] = u;
      } else {
        asm volatile("" : "+v"(pv[e]));
      }
    };
    auto pvm = [&](int t, int c) {
      if (c < 8) oacc[t][c] = mfma16(vf[c], pf[t], oacc[t][c]);
      else lsum[t] = mfma16(ones, pf[t], lsum[t]);
    };
    SB();
#pragma unroll
    for (int s = 0; s < 8; ++s) qk(0, s);
    SB();
#pragma unroll
    for (int s = 0; s < 8; ++s) {
      qk(1, s);
      if (s >= 2 && s < 6) mx(0, s - 2);
      SB();
    }
    rescale(0);
#pragma unroll
    for (int s = 0; s < 8; ++s) {
      qk(2, s);
      ldv(s);
      if (s & 1) ex(0, s >> 1);
      SB();
    }
#pragma unroll
    for (int s = 0; s < 4; ++s) { ex(0, 4 + s); mx(1, s); SB(); }
    rescale(1);
#pragma unroll
    for (int s = 0; s < 9; ++s) {
      pvm(0, s);
      if (s < 8) ex(1, s);
      if (s >= 5) mx(2, s - 5);
      SB();
    }
    rescale(2);
#pragma unroll
    for (int s = 0; s < 9; ++s) {
      pvm(1, s);
      if (s < 8) ex(2, s);
      SB();
    }
#pragma unroll
    for (int s = 0; s < 9; ++s) pvm(2, s);
    slot = (slot + 1 == rdepth) ? 0 : slot + 1;
  }

#pragma unroll
  for (int t = 0; t < 3; ++t) store_tile(a.o + ((size_t)(p * 512 + b * 32 + h) * 12 + t * 4) * 1024 + lane2 * 16, oacc[t], lsum[t]);
  asm volatile("attn_code_tail:");
}

struct OutArgs {
  const char* wo[2];
  const char* o;
  char* out;
};
constexpr int OPD = 5;
constexpr int LDS_OPROJ = 8 * OPD * 4096;
__global__ __launch_bounds__(512, 2) void oproj_kernel(OutArgs a) {
  extern __shared__ __attribute__((aligned(16))) char smem[];
  const int lane = threadIdx.x & 63;
  const int w = __builtin_amdgcn_readfirstlane(threadIdx.x >> 6);
  const int xcd = blockIdx.x & 7, j = blockIdx.x >> 3;
  const int p = xcd & 1, b = ((xcd >> 1) & 1) * 8 + (j & 7), nb = (xcd >> 2) * 4 + (j >> 3);
  const int g = lane >> 4, r = lane & 15;
  const char* wg = a.wo[p] + (size_t)w * 512 * (DM * 2) + nb * 128;
  const char* og = a.o + (size_t)(p * 512 + b * 32 + 4 * w) * 12288;
  const unsigned lw = (unsigned)(uintptr_t)(LAS char*)smem + w * (OPD * 4096);
  const unsigned so = (lane >> 3) * (DM * 2) + 16 * ((lane & 7) ^ (g << 1));
  const unsigned so1 = so + 7168, so2 = so + 2 * 7168, so3 = so + 3 * 7168, ov = lane * 16;
  const unsigned trow = 4 * g + (r >> 2);
  const unsigned tra = w * (OPD * 4096) + trow * 128 + 8 * (r & 1), tsw = ((trow >> 1) & 3) << 1, tp = (r >> 1) & 1;
  f32x4 acc[3][4];
#pragma unroll
  for (int t = 0; t < 3; ++t)
#pragma unroll
    for (int e = 0; e < 4; ++e) acc[t][e] = f32x4{0.f, 0.f, 0.f, 0.f};
  unsigned tc = 0;
  if (blockIdx.x < 8 && w == 7) tc = WARM_CODE(oproj_code_tail, lane * 128);
  u32x4 ob[OPD][3];
  auto load = [&](int s) {
    dma4c(wg + s * (32 * DM * 2), lw + (s % OPD) * 4096, so, so1, so2, so3);
    const char* os = og + (s >> 2) * 12288 + (s & 3) * 1024 + 4096;
    asm volatile(
        "global_load_dwordx4 %0, %3, %4 offset:-4096\n\tglobal_load_dwordx4 %1, %3, %4\n\tglobal_load_dwordx4 %2, %3, %5"
        : "=&v"(ob[s % OPD][0]), "=&v"(ob[s % OPD][1]), "=&v"(ob[s % OPD][2])
        : "v"(ov), "s"(os), "s"(os + 4096)
        : "memory");
  };
#pragma unroll
  for (int s = 0; s < OPD; ++s) load(s);
#pragma unroll
  for (int s = 0; s < 16; ++s) {
    const int left = (15 - s < OPD - 1) ? 15 - s : OPD - 1;
    asm volatile("s_waitcnt vmcnt(%3)" : "+v"(ob[s % OPD][0]), "+v"(ob[s % OPD][1]), "+v"(ob[s % OPD][2]) : "n"(7 * left) : "memory");
    u32x4 f[4];
#pragma unroll
    for (int e = 0; e < 4; ++e) {
      const unsigned ad = tra + (s % OPD) * 4096 + 16 * ((2 * e + tp) ^ tsw);
      s16x4 l0 = __builtin_amdgcn_ds_read_tr16_b64_v4i16((LAS s16x4*)((LAS char*)smem + ad));
      s16x4 l1 = __builtin_amdgcn_ds_read_tr16_b64_v4i16((LAS s16x4*)((LAS char*)smem + ad + 2048));
      u32x2 u0 = __builtin_bit_cast(u32x2, l0), u1 = __builtin_bit_cast(u32x2, l1);
      f[e] = u32x4{u0[0], u0[1], u1[0], u1[1]};
    }
#pragma unroll
    for (int e = 0; e < 4; ++e)
#pragma unroll
      for (int t = 0; t < 3; ++t) acc[t][e] = mfma16(f[e], ob[s % OPD][t], acc[t][e]);
    if (s + OPD < 16) {
      asm volatile("s_waitcnt lgkmcnt(0)" : "+v"(acc[2][3]) : : "memory");
      load(s + OPD);
    }
  }
  f32x4* red = (f32x4*)smem;
  constexpr int RS = OPD * 4096 / 16;
#pragma unroll
  for (int t = 0; t < 3; ++t)
#pragma unroll
    for (int e = 0; e < 4; ++e) red[w * RS + (t * 4 + e) * 64 + (((lane & 15) << 2) | (lane >> 4))] = acc[t][e];
  asm volatile("oproj_code_tail:" ::"v"(tc));
  __syncthreads();
  if (w < 6) {
    const int t = w >> 1, e = (w & 1) * 2 + (lane & 2 ? 1 : 0), q = lane >> 2, gh = lane & 1;
    f32x4 s0 = f32x4{0.f, 0.f, 0.f, 0.f}, s1 = s0;
#pragma unroll
    for (int ww = 0; ww < 8; ++ww) {
      s0 += red[ww * RS + (t * 4 + e) * 64 + q * 4 + 2 * gh];
      s1 += red[ww * RS + (t * 4 + e) * 64 + q * 4 + 2 * gh + 1];
    }
    u32x4 f = {pack2(s0[0], s0[1]), pack2(s0[2], s0[3]), pack2(s1[0], s1[1]), pack2(s1[2], s1[3])};
    *(u32x4*)(a.out + ((size_t)(b * SQ + 16 * t + q) * 1024 + p * 512 + nb * 64 + 16 * e + 8 * gh) * 2) = f;
  }
}

extern "C" void kernel_launch(void* const* d_in, const int* in_sizes, int n_in, void* d_out, int out_size, void* d_ws,
                              size_t ws_size, hipStream_t stream) {
  (void)in_sizes; (void)n_in; (void)out_size; (void)ws_size;
  AttnArgs a;
  a.x[0] = (const char*)d_in[0]; a.k[0] = (const char*)d_in[1]; a.v[0] = (const char*)d_in[2]; a.wq[0] = (const char*)d_in[3];
  a.x[1] = (const char*)d_in[5]; a.k[1] = (const char*)d_in[6]; a.v[1] = (const char*)d_in[7]; a.wq[1] = (const char*)d_in[8];
  a.wo[0] = (const char*)d_in[4]; a.wo[1] = (const char*)d_in[9];
  a.o = (char*)d_ws;
  OutArgs oa;
  oa.wo[0] = (const char*)d_in[4]; oa.wo[1] = (const char*)d_in[9]; oa.o = (const char*)d_ws; oa.out = (char*)d_out;
  (void)hipFuncSetAttribute((const void*)attn_kernel, hipFuncAttributeMaxDynamicSharedMemorySize, LDS_ATTN);
  (void)hipFuncSetAttribute((const void*)oproj_kernel, hipFuncAttributeMaxDynamicSharedMemorySize, LDS_OPROJ);
  hipLaunchKernelGGL(attn_kernel, dim3(256), dim3(512), LDS_ATTN, stream, a);
  hipLaunchKernelGGL(oproj_kernel, dim3(256), dim3(512), LDS_OPROJ, stream, oa);
}
```

```cpp
#include <hip/hip_runtime.h>
#include <hip/hip_bf16.h>
#include <stdint.h>

typedef __attribute__((ext_vector_type(8))) __bf16 bf16x8;
typedef __attribute__((ext_vector_type(2))) __bf16 bf16x2;
typedef __attribute__((ext_vector_type(4))) short s16x4;
typedef __attribute__((ext_vector_type(4))) float f32x4;
typedef __attribute__((ext_vector_type(2))) float f32x2;
typedef __attribute__((ext_vector_type(4))) unsigned u32x4;
typedef __attribute__((ext_vector_type(2))) unsigned u32x2;
#define LAS __attribute__((address_space(3)))

constexpr int SQ = 48, DM = 512, NH = 32, SK = 1024, DH = 128, HD = NH * DH;
constexpr int NBLK = SK / 32;
constexpr int BLK_BYTES = 16384;
constexpr int LDS_ATTN = 10 * BLK_BYTES;
constexpr int QOFF = 6 * BLK_BYTES;
constexpr int XOFF1 = 7 * BLK_BYTES, XOFF2 = 6 * BLK_BYTES;
constexpr int FILL_STEP = 13;
constexpr int NRB = 2, RB0 = 8, RBS = 10;
constexpr float SCL = 0.088388347648318447f * 1.4426950408889634f;
constexpr float RESCALE_THR = 6.0f;

__device__ __forceinline__ unsigned pack2(float lo, float hi) {
  f32x2 f = {lo, hi};
  return __builtin_bit_cast(unsigned, __builtin_convertvector(f, bf16x2));
}
__device__ __forceinline__ f32x4 mfma16(u32x4 a, u32x4 b, f32x4 c) {
  return __builtin_amdgcn_mfma_f32_16x16x32_bf16(__builtin_bit_cast(bf16x8, a), __builtin_bit_cast(bf16x8, b), c, 0, 0, 0);
}
__device__ __forceinline__ float xg_max(float v) {
  auto r = __builtin_amdgcn_permlane16_swap(__float_as_uint(v), __float_as_uint(v), false, false);
  v = fmaxf(__uint_as_float(r[0]), __uint_as_float(r[1]));
  auto s = __builtin_amdgcn_permlane32_swap(__float_as_uint(v), __float_as_uint(v), false, false);
  return fmaxf(__uint_as_float(s[0]), __uint_as_float(s[1]));
}
__device__ __forceinline__ float max3(float a, float b, float c) {
  return __builtin_elementwise_maximum(__builtin_elementwise_maximum(a, b), c);
}
#define SB() __builtin_amdgcn_sched_barrier(0)

#define DMA4_BODY(POL)                                                        \
  unsigned keep;                                                              \
  asm volatile(                                                               \
      "s_nop 4\n\ts_mov_b32 %0, m0\n\ts_mov_b32 m0, %1\n\ts_nop 0\n\t"      \
      "global_load_lds_dwordx4 %3, %2" POL "\n\t"                             \
      "global_load_lds_dwordx4 %4, %2 offset:1024" POL "\n\t"                 \
      "global_load_lds_dwordx4 %5, %2 offset:2048" POL "\n\t"                 \
      "global_load_lds_dwordx4 %6, %2 offset:3072" POL "\n\t"                 \
      "s_mov_b32 m0, %0"                                                      \
      : "=&s"(keep)                                                           \
      : "s"(m), "s"(g), "v"(o0), "v"(o1), "v"(o2), "v"(o3)                    \
      : "memory")
__device__ __forceinline__ void dma4(const char* g, unsigned m, unsigned o0, unsigned o1, unsigned o2, unsigned o3) { DMA4_BODY(" nt"); }
__device__ __forceinline__ void dma4c(const char* g, unsigned m, unsigned o0, unsigned o1, unsigned o2, unsigned o3) { DMA4_BODY(""); }
#define WAIT_VM(n) asm volatile("s_waitcnt vmcnt(" #n ")" ::: "memory")
#define WAIT_LGKM0() asm volatile("s_waitcnt lgkmcnt(0)" ::: "memory")
#define BAR() asm volatile("s_barrier" ::: "memory")

typedef __amdgpu_buffer_rsrc_t rsrc_t;
__device__ __forceinline__ rsrc_t mkrs(const void* p, int bytes) { return __builtin_amdgcn_make_buffer_rsrc((void*)p, (short)0, bytes, 0x00020000); }
__device__ __forceinline__ u32x4 bld128(rsrc_t rs, unsigned vo, unsigned so) { return __builtin_amdgcn_raw_buffer_load_b128(rs, vo, so, 0); }

struct AttnArgs {
  const char* x[2];
  const char* k[2];
  const char* v[2];
  const char* wq[2];
  const char* wo[2];
  char* o;
};

template <bool XLDS>
__device__ __forceinline__ void qproj(u32x4 (&qf)[3][4], const char* wq, int wbytes, const char* x, char* smem, unsigned xoff, int lane) {
  f32x4 acc[3][8];
#pragma unroll
  for (int t = 0; t < 3; ++t)
#pragma unroll
    for (int e = 0; e < 8; ++e) acc[t][e] = f32x4{0.f, 0.f, 0.f, 0.f};
  const rsrc_t rw = mkrs(wq, wbytes);
  const rsrc_t rx = mkrs(x, SQ * DM * 2);
  const unsigned g = lane >> 4, r = lane & 15;
  const unsigned wv = g * (32 * HD * 2) + r * 16;
  const unsigned xv = r * (DM * 2) + g * 64;
  const unsigned xl = xoff + r * (DM * 2) + 16 * ((4 * g) ^ (((r & 3) << 2) | (r >> 2)));
  constexpr int XA = XLDS ? 1 : 2;
  u32x4 wb[3][8], xb[XA + 1][3];
  auto loadw = [&](int s, int jj) { wb[s % 3][jj] = bld128(rw, wv, (128 * (s >> 2) + 8 * (s & 3) + jj) * (HD * 2)); };
  auto loadx = [&](int s, int t) {
    if (XLDS) xb[s % (XA + 1)][t] = *(const LAS u32x4*)((LAS char*)smem + (xl ^ (16 * (s & 3))) + t * 16 * DM * 2 + (s >> 2) * 256);
    else xb[s % (XA + 1)][t] = bld128(rx, xv + (s >> 2) * 256 + (s & 3) * 16, t * 16 * DM * 2);
  };
  u32x4 f[2];
  auto tr = [&](int n, int t) {
    const int s = n >> 3, e = n & 7;
    f[n & 1][t] = __builtin_amdgcn_perm(wb[s % 3][2 * t + 1][e >> 1], wb[s % 3][2 * t][e >> 1], (e & 1) ? 0x07060302u : 0x05040100u);
  };
#pragma unroll
  for (int s = 0; s < 2; ++s) {
#pragma unroll
    for (int jj = 0; jj < 8; ++jj) loadw(s, jj);
    if (!XLDS)
#pragma unroll
      for (int t = 0; t < 3; ++t) loadx(s, t);
  }
  BAR();
  if (XLDS)
#pragma unroll
    for (int t = 0; t < 3; ++t) loadx(0, t);
#pragma unroll
  for (int t = 0; t < 4; ++t) tr(0, t);
#pragma unroll
  for (int s = 0; s < 16; ++s)
#pragma unroll
  for (int e = 0; e < 8; ++e) {
    const int n = 8 * s + e;
    if (n == 8 * FILL_STEP + 4) BAR();
    SB();
    acc[0][e] = mfma16(f[n & 1], xb[s % (XA + 1)][0], acc[0][e]);
    if (n + 1 < 128) { tr(n + 1, 0); tr(n + 1, 1); }
    SB();
    acc[1][e] = mfma16(f[n & 1], xb[s % (XA + 1)][1], acc[1][e]);
    if (n + 1 < 128) { tr(n + 1, 2); tr(n + 1, 3); }
    SB();
    acc[2][e] = mfma16(f[n & 1], xb[s % (XA + 1)][2], acc[2][e]);
    if (e < 4 && s + 2 < 16) { loadw(s + 2, 2 * e); loadw(s + 2, 2 * e + 1); }
    if (e >= 4 && e < 7 && s + XA < 16) loadx(s + XA, e - 4);
  }
  SB();
#pragma unroll
  for (int t = 0; t < 3; ++t)
#pragma unroll
    for (int s = 0; s < 4; ++s)
#pragma unroll
      for (int e = 0; e < 4; ++e) qf[t][s][e] = pack2(acc[t][2 * e][s], acc[t][2 * e + 1][s]);
}

__device__ __forceinline__ void store_tile(char* op, const f32x4 (&o)[8], const f32x4& l) {
  const float inv = 1.0f / l[0];
#pragma unroll
  for (int m = 0; m < 4; ++m) {
    u32x4 f;
    f[0] = pack2(o[2 * m][0] * inv, o[2 * m][1] * inv);
    f[1] = pack2(o[2 * m][2] * inv, o[2 * m][3] * inv);
    f[2] = pack2(o[2 * m + 1][0] * inv, o[2 * m + 1][1] * inv);
    f[3] = pack2(o[2 * m + 1][2] * inv, o[2 * m + 1][3] * inv);
    asm volatile("global_store_dwordx4 %0, %1, off sc0 sc1\n\ts_nop 2" ::"v"(op + m * 1024), "v"(f) : "memory");
  }
}

__global__ __launch_bounds__(512, 2) void attn_kernel(AttnArgs a) {
  extern __shared__ __attribute__((aligned(16))) char smem[];
  const int lane = threadIdx.x & 63;
  const int w = __builtin_amdgcn_readfirstlane(threadIdx.x >> 6);
  const int xcd = blockIdx.x & 7, j = blockIdx.x >> 3;
  const bool type1 = j < 16;
  const int b = j & 15;
  const unsigned lds0 = (unsigned)(uintptr_t)(LAS char*)smem;
  const int nst = type1 ? 2 : 3, rdepth = type1 ? 5 : 3;
  const unsigned xoff = type1 ? XOFF1 : XOFF2;

  if (w >= (type1 ? 5 : 4)) {
    const unsigned g = lane >> 4, r = lane & 15;
    unsigned tq[8];
#pragma unroll
    for (int q = 0; q < 8; ++q) tq[q] = 0;
    if (type1 && w == 5) {
      const unsigned vo = (unsigned)((32 * b + (lane >> 3)) * (HD * 2) + xcd * 1024 + (lane & 7) * 128);
      const unsigned v1 = vo + 8u * HD * 2, v2 = vo + 16u * HD * 2, v3 = vo + 24u * HD * 2;
      asm volatile(
          "s_nop 4\n\t"
          "global_load_dword %0, %8, %13\n\tglobal_load_dword %1, %9, %13\n\tglobal_load_dword %2, %10, %13\n\tglobal_load_dword %3, %11, %13\n\t"
          "global_load_dword %4, %8, %12\n\tglobal_load_dword %5, %9, %12\n\tglobal_load_dword %6, %10, %12\n\tglobal_load_dword %7, %11, %12"
          : "=&v"(tq[0]), "=&v"(tq[1]), "=&v"(tq[2]), "=&v"(tq[3]), "=&v"(tq[4]), "=&v"(tq[5]), "=&v"(tq[6]), "=&v"(tq[7])
          : "v"(vo), "v"(v1), "v"(v2), "v"(v3), "s"(a.wq[0]), "s"(a.wq[1])
          : "memory");
    }
    const int li = 7 - w;
    {
      const char* xg = a.x[type1 ? 0 : 1] + (size_t)b * (SQ * DM * 2);
      const int nq = type1 ? 4 : 3;
      for (int q = 0; q < nq; ++q) {
        const int rg = li * nq + q;
        const unsigned base = g * 256 + 16 * (r ^ (unsigned)(rg & 3));
        dma4c(xg + rg * 4096, lds0 + xoff + rg * 4096, base, base ^ 64u, base ^ 128u, base ^ 192u);
      }
    }
    const int nu = (type1 && li == 2) ? 2 : 3;
    const char* up[3];
    unsigned ul[3], uo[3][4];
    bool uA[3];
#pragma unroll
    for (int u = 0; u < 3; ++u) {
      const int id = (li * 3 + u) & (type1 ? 7 : 15);
      const int st = id >> 2, isv = (id >> 1) & 1, hf = id & 1;
      uA[u] = type1 && st == 1;
      const size_t sidx = uA[u] ? (size_t)(b * 8 + xcd) : (size_t)(b * 32 + 4 * xcd + (type1 ? 0 : 1 + st));
      const char* kb = uA[u] ? a.k[0] : a.k[1];
      const char* vb = uA[u] ? a.v[0] : a.v[1];
      up[u] = (isv ? vb : kb) + sidx * (SK * DH * 2) + hf * 4096;
      ul[u] = lds0 + st * BLK_BYTES + isv * 8192 + hf * 4096;
#pragma unroll
      for (int q = 0; q < 4; ++q) uo[u][q] = g * 256 + 16 * (r ^ (isv ? (((q & 1) * 4 + g) << 1) : ((g << 2) | q)));
    }
    const unsigned sstride = nst * BLK_BYTES;
    auto issue = [&](int blk, int slot, int which) {
#pragma unroll
      for (int u = 0; u < 3; ++u)
        if (u < nu && (which == 0 || (which == 2) == uA[u])) dma4(up[u] + blk * 8192, ul[u] + slot * sstride, uo[u][0], uo[u][1], uo[u][2], uo[u][3]);
    };
    int slot = rdepth - 1;
    if (type1) {
      issue(0, 0, 0); issue(1, 1, 0); issue(2, 2, 0);
      u32x4 rb[NRB][3][4];
#pragma unroll
      for (int u = 0; u < 3; ++u) {
        const rsrc_t rs = mkrs(up[u < nu ? u : 0], SK * DH * 2 - 4096);
#pragma unroll
        for (int k = 0; k < NRB; ++k)
#pragma unroll
          for (int q = 0; q < 4; ++q)
            rb[k][u][q] = (u < nu) ? __builtin_amdgcn_raw_buffer_load_b128(rs, uo[u][q] + q * 1024, (RB0 + k * RBS) * 8192, 2) : u32x4{0, 0, 0, 0};
      }
      asm volatile("s_waitcnt vmcnt(%0)" ::"n"(8 * NRB) : "memory");
      BAR();
      BAR();
      WAIT_VM(0);
#pragma unroll
      for (int k = 0; k < NRB; ++k)
#pragma unroll
        for (int u = 0; u < 3; ++u) asm volatile("" : "+v"(rb[k][u][0]), "+v"(rb[k][u][1]), "+v"(rb[k][u][2]), "+v"(rb[k][u][3]));
      auto isreg = [&](int blk) { return blk >= RB0 && blk < RB0 + NRB * RBS && (blk - RB0) % RBS == 0; };
      u32x4 q1[4];
      f32x4 o1[8], ls = f32x4{0.f, 0.f, 0.f, 0.f};
#pragma unroll
      for (int c = 0; c < 8; ++c) o1[c] = f32x4{0.f, 0.f, 0.f, 0.f};
      float mr = -__builtin_inff(), th = -__builtin_inff();
      u32x4 ones = {0x3f803f80u, 0x3f803f80u, 0x3f803f80u, 0x3f803f80u};
      asm volatile("" : "+v"(ones));
      const unsigned fk = ((r & 3) << 2) | (r >> 2), vrow = 4 * g + (r >> 2);
      const unsigned vra = 8192 + vrow * 256 + 8 * (r & 1), vsw = (vrow & 7) << 1, vp = (r >> 1) & 1;
      int cs = 0;
      for (int i = 0; i < NBLK; ++i) {
        int cnt = (i == 3) ? -1 : 0;
#pragma unroll
        for (int d = 1; d <= 3; ++d) cnt += (i + d < NBLK && !isreg(i + d)) ? 1 : 0;
        if (nu == 3) { if (cnt == 3) WAIT_VM(36); else if (cnt == 2) WAIT_VM(24); else if (cnt == 1) WAIT_VM(12); else WAIT_VM(0); }
        else { if (cnt == 3) WAIT_VM(24); else if (cnt == 2) WAIT_VM(16); else if (cnt == 1) WAIT_VM(8); else WAIT_VM(0); }
        BAR();
        if (i == 0) {
#pragma unroll
          for (int c = 0; c < 4; ++c) q1[c] = *(const LAS u32x4*)((LAS char*)smem + QOFF + (li * 4 + c) * 1024 + lane * 16);
          issue(3, 3, 2);
        }
        if (i == 1) issue(3, 3, 1);
        if (isreg(i + 4)) {
#pragma unroll
          for (int k = 0; k < NRB; ++k)
            if (i + 4 == RB0 + k * RBS) {
#pragma unroll
              for (int u = 0; u < 3; ++u)
                if (u < nu)
#pragma unroll
                  for (int q = 0; q < 4; ++q)
                    *(LAS u32x4*)(uintptr_t)(ul[u] + slot * sstride + q * 1024 + lane * 16) = rb[k][u][q];
            }
          WAIT_LGKM0();
        } else if (i + 4 < NBLK) {
          issue(i + 4, slot, 0);
        }
        slot = (slot + 1 == rdepth) ? 0 : slot + 1;
        const unsigned sb = cs * sstride;
        f32x4 sc[2] = {f32x4{0.f, 0.f, 0.f, 0.f}, f32x4{0.f, 0.f, 0.f, 0.f}};
#pragma unroll
        for (int c = 0; c < 8; ++c) {
          const u32x4 kf = *(const LAS u32x4*)((LAS char*)smem + sb + r * 256 + 16 * ((4 * g + (c & 3)) ^ fk) + (c >> 2) * 4096);
          sc[c >> 2] = mfma16(kf, q1[c & 3], sc[c >> 2]);
        }
        u32x4 vf[8], pf;
#pragma unroll
        for (int c = 0; c < 8; ++c) {
          const unsigned ad = sb + vra + 16 * ((2 * c + vp) ^ vsw);
          s16x4 l0 = __builtin_amdgcn_ds_read_tr16_b64_v4i16((LAS s16x4*)((LAS char*)smem + ad));
          s16x4 l1 = __builtin_amdgcn_ds_read_tr16_b64_v4i16((LAS s16x4*)((LAS char*)smem + ad + 4096));
          u32x2 u0 = __builtin_bit_cast(u32x2, l0), u1 = __builtin_bit_cast(u32x2, l1);
          vf[c] = u32x4{u0[0], u0[1], u1[0], u1[1]};
        }
        float lm = max3(max3(sc[0][0], sc[0][1], sc[0][2]), sc[0][3], sc[1][0]);
        lm = __builtin_elementwise_maximum(max3(lm, sc[1][1], sc[1][2]), sc[1][3]);
        if (__builtin_expect(__any(!(lm <= th)), 0)) {
          const float mn = fmaxf(mr, xg_max(lm) * SCL);
          const float al = __builtin_amdgcn_exp2f(mr - mn);
          mr = mn;
          th = (mn + RESCALE_THR) * (1.0f / SCL);
          ls *= al;
#pragma unroll
          for (int c = 0; c < 8; ++c) o1[c] *= al;
        }
#pragma unroll
        for (int e = 0; e < 4; ++e)
          pf[e] = pack2(__builtin_amdgcn_exp2f(fmaf(sc[e >> 1][2 * (e & 1)], SCL, -mr)), __builtin_amdgcn_exp2f(fmaf(sc[e >> 1][2 * (e & 1) + 1], SCL, -mr)));
#pragma unroll
        for (int c = 0; c < 8; ++c) o1[c] = mfma16(vf[c], pf, o1[c]);
        ls = mfma16(ones, pf, ls);
        cs = (cs + 1 == rdepth) ? 0 : cs + 1;
        WAIT_LGKM0();
      }
      store_tile(a.o + ((size_t)(512 + b * 32 + 4 * xcd) * 12 + li * 4) * 1024 + lane * 16, o1, ls);
    } else {
      WAIT_VM(0);
      BAR();
      BAR();
      issue(0, 0, 0); issue(1, 1, 0);
      for (int i = 0; i < NBLK; ++i) {
        if (i + 1 < NBLK) WAIT_VM(12); else WAIT_VM(0);
        BAR();
        if (i + 2 < NBLK) issue(i + 2, slot, 0);
        slot = (slot + 1 == rdepth) ? 0 : slot + 1;
      }
    }
#pragma unroll
    for (int q = 0; q < 8; ++q) asm volatile("" ::"v"(tq[q]));
    return;
  }
  if (!type1 && w == 3) {
    for (int i = 0; i < NBLK + 2; ++i) BAR();
    const rsrc_t rw = mkrs(a.wo[xcd & 1], HD * DM * 2);
    const unsigned vo = (unsigned)((256 * b + (lane >> 2)) * (DM * 2) + (xcd >> 2) * 512 + (lane & 3) * 128);
    unsigned t[16];
#pragma unroll
    for (int q = 0; q < 16; ++q) t[q] = __builtin_amdgcn_raw_buffer_load_b32(rw, vo, q * 16 * (DM * 2), 0);
#pragma unroll
    for (int q = 0; q < 16; ++q) asm volatile("" ::"v"(t[q]));
    return;
  }

  const bool roleA = type1 && w < 4;
  const int p = roleA ? 0 : 1;
  const int h = roleA ? 4 * xcd + w : (type1 ? 4 * xcd : 4 * xcd + 1 + w);
  const unsigned ringoff = (unsigned)((type1 ? (roleA ? 1 : 0) : w) * BLK_BYTES);
  const bool xlds = !(type1 && w == 4);

  u32x4 qf[3][4];
  if (xlds) qproj<true>(qf, a.wq[p] + h * 256, DM * HD * 2 - h * 256, a.x[p] + b * (SQ * DM * 2), smem, xoff, lane);
  else qproj<false>(qf, a.wq[p] + h * 256, DM * HD * 2 - h * 256, a.x[p] + b * (SQ * DM * 2), smem, xoff, lane);
  if (!xlds) {
#pragma unroll
    for (int c = 0; c < 12; ++c) *(LAS u32x4*)((LAS char*)smem + QOFF + c * 1024 + lane * 16) = qf[c >> 2][c & 3];
    WAIT_LGKM0();
    BAR();
    return;
  }

  int lane2 = lane;
  asm volatile("" : "+v"(lane2));
  const int g = lane2 >> 4, r = lane2 & 15;
  f32x4 oacc[3][8];
#pragma unroll
  for (int t = 0; t < 3; ++t)
#pragma unroll
    for (int c = 0; c < 8; ++c) oacc[t][c] = f32x4{0.f, 0.f, 0.f, 0.f};
  float mrow[3] = {-__builtin_inff(), -__builtin_inff(), -__builtin_inff()};
  float thr[3] = {-__builtin_inff(), -__builtin_inff(), -__builtin_inff()};
  f32x4 lsum[3] = {f32x4{0.f, 0.f, 0.f, 0.f}, f32x4{0.f, 0.f, 0.f, 0.f}, f32x4{0.f, 0.f, 0.f, 0.f}};
  u32x4 ones = {0x3f803f80u, 0x3f803f80u, 0x3f803f80u, 0x3f803f80u};
  asm volatile("" : "+v"(ones));
  const unsigned fk = ((r & 3) << 2) | (r >> 2);
  unsigned kra[4];
#pragma unroll
  for (int s = 0; s < 4; ++s) kra[s] = r * 256 + 16 * ((4 * g + s) ^ fk);
  const unsigned vrow = 4 * g + (r >> 2);
  const unsigned vra = 8192 + vrow * 256 + 8 * (r & 1);
  const unsigned vsw = (vrow & 7) << 1, vp = (r >> 1) & 1;

  int slot = 0;
  for (int i = 0; i < NBLK; ++i) {
    const unsigned sb = ringoff + slot * nst * BLK_BYTES;
    WAIT_LGKM0();
    BAR();
    f32x4 sc[3][2];
    u32x4 kf[8], vf[8], pf[3];
    float lm = 0.f, pv[8];
#pragma unroll
    for (int s = 0; s < 8; ++s) kf[s] = *(const LAS u32x4*)((LAS char*)smem + sb + kra[s & 3] + (s >> 2) * 4096);
    auto qk = [&](int t, int s) {
      sc[t][s >> 2] = mfma16(kf[s], qf[t][s & 3], (s & 3) ? sc[t][s >> 2] : f32x4{0.f, 0.f, 0.f, 0.f});
    };
    auto ldv = [&](int c) {
      const unsigned ad = sb + vra + 16 * ((2 * c + vp) ^ vsw);
      s16x4 l0 = __builtin_amdgcn_ds_read_tr16_b64_v4i16((LAS s16x4*)((LAS char*)smem + ad));
      s16x4 l1 = __builtin_amdgcn_ds_read_tr16_b64_v4i16((LAS s16x4*)((LAS char*)smem + ad + 4096));
      u32x2 u0 = __builtin_bit_cast(u32x2, l0), u1 = __builtin_bit_cast(u32x2, l1);
      vf[c] = u32x4{u0[0], u0[1], u1[0], u1[1]};
    };
    auto mx = [&](int t, int q) {
      if (q == 0) lm = max3(sc[t][0][0], sc[t][0][1], sc[t][0][2]);
      else if (q == 1) lm = max3(lm, sc[t][0][3], sc[t][1][0]);
      else if (q == 2) lm = max3(lm, sc[t][1][1], sc[t][1][2]);
      else lm = __builtin_elementwise_maximum(lm, sc[t][1][3]);
    };
    auto rescale = [&](int t) {
      if (__builtin_expect(__any(!(lm <= thr[t])), 0)) {
        const float mn = fmaxf(mrow[t], xg_max(lm) * SCL);
        const float al = __builtin_amdgcn_exp2f(mrow[t] - mn);
        mrow[t] = mn;
        thr[t] = (mn + RESCALE_THR) * (1.0f / SCL);
        lsum[t] *= al;
#pragma unroll
        for (int c = 0; c < 8; ++c) oacc[t][c] *= al;
      }
    };
    auto ex = [&](int t, int e) {
      pv[e] = __builtin_amdgcn_exp2f(fmaf(sc[t][e >> 2][e & 3], SCL, -mrow[t]));
      if (e & 1) {
        unsigned u = pack2(pv[e - 1], pv[e]);
        asm volatile("" : "+v"(u));
        pf[t][e >> 1] = u;
      } else {
        asm volatile("" : "+v"(pv[e]));
      }
    };
    auto pvm = [&](int t, int c) {
      if (c < 8) oacc[t][c] = mfma16(vf[c], pf[t], oacc[t][c]);
      else lsum[t] = mfma16(ones, pf[t], lsum[t]);
    };
    SB();
#pragma unroll
    for (int s = 0; s < 8; ++s) qk(0, s);
    SB();
#pragma unroll
    for (int s = 0; s < 8; ++s) {
      qk(1, s);
      if (s >= 2 && s < 6) mx(0, s - 2);
      SB();
    }
    rescale(0);
#pragma unroll
    for (int s = 0; s < 8; ++s) {
      qk(2, s);
      ldv(s);
      if (s & 1) ex(0, s >> 1);
      SB();
    }
#pragma unroll
    for (int s = 0; s < 4; ++s) { ex(0, 4 + s); mx(1, s); SB(); }
    rescale(1);
#pragma unroll
    for (int s = 0; s < 9; ++s) {
      pvm(0, s);
      if (s < 8) ex(1, s);
      if (s >= 5) mx(2, s - 5);
      SB();
    }
    rescale(2);
#pragma unroll
    for (int s = 0; s < 9; ++s) {
      pvm(1, s);
      if (s < 8) ex(2, s);
      SB();
    }
#pragma unroll
    for (int s = 0; s < 9; ++s) pvm(2, s);
    slot = (slot + 1 == rdepth) ? 0 : slot + 1;
  }

#pragma unroll
  for (int t = 0; t < 3; ++t) store_tile(a.o + ((size_t)(p * 512 + b * 32 + h) * 12 + t * 4) * 1024 + lane2 * 16, oacc[t], lsum[t]);
}

struct OutArgs {
  const char* wo[2];
  const char* o;
  char* out;
};
constexpr int OPD = 5;
constexpr int LDS_OPROJ = 8 * OPD * 4096;
__global__ __launch_bounds__(512, 2) void oproj_kernel(OutArgs a) {
  extern __shared__ __attribute__((aligned(16))) char smem[];
  const int lane = threadIdx.x & 63;
  const int w = __builtin_amdgcn_readfirstlane(threadIdx.x >> 6);
  const int xcd = blockIdx.x & 7, j = blockIdx.x >> 3;
  const int p = xcd & 1, b = ((xcd >> 1) & 1) * 8 + (j & 7), nb = (xcd >> 2) * 4 + (j >> 3);
  const int g = lane >> 4, r = lane & 15;
  const char* wg = a.wo[p] + (size_t)w * 512 * (DM * 2) + nb * 128;
  const char* og = a.o + (size_t)(p * 512 + b * 32 + 4 * w) * 12288;
  const unsigned lw = (unsigned)(uintptr_t)(LAS char*)smem + w * (OPD * 4096);
  const unsigned so = (lane >> 3) * (DM * 2) + 16 * ((lane & 7) ^ (g << 1));
  const unsigned so1 = so + 7168, so2 = so + 2 * 7168, so3 = so + 3 * 7168, ov = lane * 16;
  const unsigned trow = 4 * g + (r >> 2);
  const unsigned tra = w * (OPD * 4096) + trow * 128 + 8 * (r & 1), tsw = ((trow >> 1) & 3) << 1, tp = (r >> 1) & 1;
  f32x4 acc[3][4];
#pragma unroll
  for (int t = 0; t < 3; ++t)
#pragma unroll
    for (int e = 0; e < 4; ++e) acc[t][e] = f32x4{0.f, 0.f, 0.f, 0.f};
  u32x4 ob[OPD][3];
  auto load = [&](int s) {
    dma4c(wg + s * (32 * DM * 2), lw + (s % OPD) * 4096, so, so1, so2, so3);
    const char* os = og + (s >> 2) * 12288 + (s & 3) * 1024 + 4096;
    asm volatile(
        "global_load_dwordx4 %0, %3, %4 offset:-4096\n\tglobal_load_dwordx4 %1, %3, %4\n\tglobal_load_dwordx4 %2, %3, %5"
        : "=&v"(ob[s % OPD][0]), "=&v"(ob[s % OPD][1]), "=&v"(ob[s % OPD][2])
        : "v"(ov), "s"(os), "s"(os + 4096)
        : "memory");
  };
#pragma unroll
  for (int s = 0; s < OPD; ++s) load(s);
#pragma unroll
  for (int s = 0; s < 16; ++s) {
    const int left = (15 - s < OPD - 1) ? 15 - s : OPD - 1;
    asm volatile("s_waitcnt vmcnt(%3)" : "+v"(ob[s % OPD][0]), "+v"(ob[s % OPD][1]), "+v"(ob[s % OPD][2]) : "n"(7 * left) : "memory");
    u32x4 f[4];
#pragma unroll
    for (int e = 0; e < 4; ++e) {
      const unsigned ad = tra + (s % OPD) * 4096 + 16 * ((2 * e + tp) ^ tsw);
      s16x4 l0 = __builtin_amdgcn_ds_read_tr16_b64_v4i16((LAS s16x4*)((LAS char*)smem + ad));
      s16x4 l1 = __builtin_amdgcn_ds_read_tr16_b64_v4i16((LAS s16x4*)((LAS char*)smem + ad + 2048));
      u32x2 u0 = __builtin_bit_cast(u32x2, l0), u1 = __builtin_bit_cast(u32x2, l1);
      f[e] = u32x4{u0[0], u0[1], u1[0], u1[1]};
    }
#pragma unroll
    for (int e = 0; e < 4; ++e)
#pragma unroll
      for (int t = 0; t < 3; ++t) acc[t][e] = mfma16(f[e], ob[s % OPD][t], acc[t][e]);
    if (s + OPD < 16) {
      asm volatile("s_waitcnt lgkmcnt(0)" : "+v"(acc[2][3]) : : "memory");
      load(s + OPD);
    }
  }
  f32x4* red = (f32x4*)smem;
  constexpr int RS = OPD * 4096 / 16;
#pragma unroll
  for (int t = 0; t < 3; ++t)
#pragma unroll
    for (int e = 0; e < 4; ++e) red[w * RS + (t * 4 + e) * 64 + (((lane & 15) << 2) | (lane >> 4))] = acc[t][e];
  __syncthreads();
  if (w < 6) {
    const int t = w >> 1, e = (w & 1) * 2 + (lane & 2 ? 1 : 0), q = lane >> 2, gh = lane & 1;
    f32x4 s0 = f32x4{0.f, 0.f, 0.f, 0.f}, s1 = s0;
#pragma unroll
    for (int ww = 0; ww < 8; ++ww) {
      s0 += red[ww * RS + (t * 4 + e) * 64 + q * 4 + 2 * gh];
      s1 += red[ww * RS + (t * 4 + e) * 64 + q * 4 + 2 * gh + 1];
    }
    u32x4 f = {pack2(s0[0], s0[1]), pack2(s0[2], s0[3]), pack2(s1[0], s1[1]), pack2(s1[2], s1[3])};
    *(u32x4*)(a.out + ((size_t)(b * SQ + 16 * t + q) * 1024 + p * 512 + nb * 64 + 16 * e + 8 * gh) * 2) = f;
  }
}

extern "C" void kernel_launch(void* const* d_in, const int* in_sizes, int n_in, void* d_out, int out_size, void* d_ws,
                              size_t ws_size, hipStream_t stream) {
  (void)in_sizes; (void)n_in; (void)out_size; (void)ws_size;
  AttnArgs a;
  a.x[0] = (const char*)d_in[0]; a.k[0] = (const char*)d_in[1]; a.v[0] = (const char*)d_in[2]; a.wq[0] = (const char*)d_in[3];
  a.x[1] = (const char*)d_in[5]; a.k[1] = (const char*)d_in[6]; a.v[1] = (const char*)d_in[7]; a.wq[1] = (const char*)d_in[8];
  a.wo[0] = (const char*)d_in[4]; a.wo[1] = (const char*)d_in[9];
  a.o = (char*)d_ws;
  OutArgs oa;
  oa.wo[0] = (const char*)d_in[4]; oa.wo[1] = (const char*)d_in[9]; oa.o = (const char*)d_ws; oa.out = (char*)d_out;
  (void)hipFuncSetAttribute((const void*)attn_kernel, hipFuncAttributeMaxDynamicSharedMemorySize, LDS_ATTN);
  (void)hipFuncSetAttribute((const void*)oproj_kernel, hipFuncAttributeMaxDynamicSharedMemorySize, LDS_OPROJ);
  hipLaunchKernelGGL(attn_kernel, dim3(256), dim3(512), LDS_ATTN, stream, a);
  hipLaunchKernelGGL(oproj_kernel, dim3(256), dim3(512), LDS_OPROJ, stream, oa);
}
```

```cpp
#include <hip/hip_runtime.h>
#include <hip/hip_bf16.h>
#include <stdint.h>

typedef __attribute__((ext_vector_type(8))) __bf16 bf16x8;
typedef __attribute__((ext_vector_type(2))) __bf16 bf16x2;
typedef __attribute__((ext_vector_type(4))) short s16x4;
typedef __attribute__((ext_vector_type(4))) float f32x4;
typedef __attribute__((ext_vector_type(2))) float f32x2;
typedef __attribute__((ext_vector_type(4))) unsigned u32x4;
typedef __attribute__((ext_vector_type(2))) unsigned u32x2;
#define LAS __attribute__((address_space(3)))

#define KV_NT 1
#define NT_STR " nt"

constexpr int SQ = 48, DM = 512, NH = 32, SK = 1024, DH = 128, HD = NH * DH;
constexpr int NBLK = SK / 32;
constexpr int BLK_BYTES = 16384;
constexpr int LDS_ATTN = 10 * BLK_BYTES;
constexpr int XOFF1 = 7 * BLK_BYTES, XOFF2 = 6 * BLK_BYTES;
#define FILL_STEP 13
#define QSTAMP(n)
#define FILL1_LATE 0
#define NO_WQ_PF 0
#define WO_PF_IT 31
#define WO_GRAN 128
#define WO_PER 64
#define X_NT 0
#define WQ_AUX 0
#define WQPF_AUX 0
#define NRB 4
#define RB0 5
#define RBS 5
#define KPH 0
#define WO_MATCH 1
#define WQ_PF3 2
constexpr float SCL = 0.088388347648318447f * 1.4426950408889634f;
constexpr float RESCALE_THR = 6.0f;

__device__ __forceinline__ unsigned pack2(float lo, float hi) {
  f32x2 f = {lo, hi};
  return __builtin_bit_cast(unsigned, __builtin_convertvector(f, bf16x2));
}
__device__ __forceinline__ f32x4 mfma16(u32x4 a, u32x4 b, f32x4 c) {
  return __builtin_amdgcn_mfma_f32_16x16x32_bf16(__builtin_bit_cast(bf16x8, a), __builtin_bit_cast(bf16x8, b), c, 0, 0, 0);
}
__device__ __forceinline__ float xg_max(float v) {
  auto r = __builtin_amdgcn_permlane16_swap(__float_as_uint(v), __float_as_uint(v), false, false);
  v = fmaxf(__uint_as_float(r[0]), __uint_as_float(r[1]));
  auto s = __builtin_amdgcn_permlane32_swap(__float_as_uint(v), __float_as_uint(v), false, false);
  return fmaxf(__uint_as_float(s[0]), __uint_as_float(s[1]));
}
__device__ __forceinline__ float max3(float a, float b, float c) {
  return __builtin_elementwise_maximum(__builtin_elementwise_maximum(a, b), c);
}
#define SB() __builtin_amdgcn_sched_barrier(0)

#define DMA4_BODY(POL)                                                        \
  unsigned keep;                                                              \
  asm volatile(                                                               \
      "s_nop 4\n\ts_mov_b32 %0, m0\n\ts_mov_b32 m0, %1\n\ts_nop 0\n\t"      \
      "global_load_lds_dwordx4 %3, %2" POL "\n\t"                             \
      "global_load_lds_dwordx4 %4, %2 offset:1024" POL "\n\t"                 \
      "global_load_lds_dwordx4 %5, %2 offset:2048" POL "\n\t"                 \
      "global_load_lds_dwordx4 %6, %2 offset:3072" POL "\n\t"                 \
      "s_mov_b32 m0, %0"                                                      \
      : "=&s"(keep)                                                           \
      : "s"(m), "s"(g), "v"(o0), "v"(o1), "v"(o2), "v"(o3)                    \
      : "memory")
__device__ __forceinline__ void dma4(const char* g, unsigned m, unsigned o0, unsigned o1, unsigned o2, unsigned o3) { DMA4_BODY(NT_STR); }
__device__ __forceinline__ void dma4c(const char* g, unsigned m, unsigned o0, unsigned o1, unsigned o2, unsigned o3) {
  DMA4_BODY("");
}
#define WAIT_VM(n) asm volatile("s_waitcnt vmcnt(" #n ")" ::: "memory")
#define WAIT_LGKM0() asm volatile("s_waitcnt lgkmcnt(0)" ::: "memory")
#define BAR() asm volatile("s_barrier" ::: "memory")

typedef __amdgpu_buffer_rsrc_t rsrc_t;
__device__ __forceinline__ rsrc_t mkrs(const void* p, int bytes) { return __builtin_amdgcn_make_buffer_rsrc((void*)p, (short)0, bytes, 0x00020000); }
__device__ __forceinline__ u32x4 bld128(rsrc_t rs, unsigned vo, unsigned so) { return __builtin_amdgcn_raw_buffer_load_b128(rs, vo, so, 0); }
__device__ __forceinline__ u32x2 bld64(rsrc_t rs, unsigned vo, unsigned so) { return __builtin_amdgcn_raw_buffer_load_b64(rs, vo, so, 0); }

struct AttnArgs {
  const char* x[2];
  const char* k[2];
  const char* v[2];
  const char* wq[2];
  const char* wo[2];
  char* o;
};

template <bool XLDS>
__device__ __forceinline__ void qproj(u32x4 (&qf)[3][4], const char* wq, int wbytes, const char* x, char* smem, unsigned xoff, int lane) {
  f32x4 acc[3][8];
#pragma unroll
  for (int t = 0; t < 3; ++t)
#pragma unroll
    for (int e = 0; e < 8; ++e) acc[t][e] = f32x4{0.f, 0.f, 0.f, 0.f};
  const rsrc_t rw = mkrs(wq, wbytes);
  const rsrc_t rx = mkrs(x, SQ * DM * 2);
  const unsigned g = lane >> 4, r = lane & 15;
  const unsigned wv = g * (32 * HD * 2) + r * 16;
  const unsigned xv = r * (DM * 2) + g * 64;
  const unsigned xl = xoff + r * (DM * 2) + 16 * ((4 * g) ^ (((r & 3) << 2) | (r >> 2)));
  constexpr int XA = XLDS ? 1 : 2;
  u32x4 wb[3][8], xb[XA + 1][3];
  auto loadw = [&](int s, int jj) { wb[s % 3][jj] = bld128(rw, wv, (128 * (s >> 2) + 8 * (s & 3) + jj) * (HD * 2)); };
  auto loadx = [&](int s, int t) {
    if (XLDS) xb[s % (XA + 1)][t] = *(const LAS u32x4*)((LAS char*)smem + (xl ^ (16 * (s & 3))) + t * 16 * DM * 2 + (s >> 2) * 256);
    else xb[s % (XA + 1)][t] = bld128(rx, xv + (s >> 2) * 256 + (s & 3) * 16, t * 16 * DM * 2);
  };
  u32x4 f[2];
  auto tr = [&](int n, int t) {
    const int s = n >> 3, e = n & 7;
    f[n & 1][t] = __builtin_amdgcn_perm(wb[s % 3][2 * t + 1][e >> 1], wb[s % 3][2 * t][e >> 1], (e & 1) ? 0x07060302u : 0x05040100u);
  };
#pragma unroll
  for (int s = 0; s < 2; ++s) {
#pragma unroll
    for (int jj = 0; jj < 8; ++jj) loadw(s, jj);
    if (!XLDS)
#pragma unroll
      for (int t = 0; t < 3; ++t) loadx(s, t);
  }
  BAR();
  QSTAMP(0);
  if (XLDS)
#pragma unroll
    for (int t = 0; t < 3; ++t) loadx(0, t);
#pragma unroll
  for (int t = 0; t < 4; ++t) tr(0, t);
#pragma unroll
  for (int s = 0; s < 16; ++s)
#pragma unroll
  for (int e = 0; e < 8; ++e) {
    const int n = 8 * s + e;
    if (n == 8 * FILL_STEP + 4) BAR();
    SB();
    acc[0][e] = mfma16(f[n & 1], xb[s % (XA + 1)][0], acc[0][e]);
    if (n + 1 < 128) { tr(n + 1, 0); tr(n + 1, 1); }
    SB();
    acc[1][e] = mfma16(f[n & 1], xb[s % (XA + 1)][1], acc[1][e]);
    if (n + 1 < 128) { tr(n + 1, 2); tr(n + 1, 3); }
    SB();
    acc[2][e] = mfma16(f[n & 1], xb[s % (XA + 1)][2], acc[2][e]);
    if (e < 4 && s + 2 < 16) { loadw(s + 2, 2 * e); loadw(s + 2, 2 * e + 1); }
    if (e >= 4 && e < 7 && s + XA < 16) loadx(s + XA, e - 4);
    if (n == 7) QSTAMP(1);
    if (n == 63) QSTAMP(2);
  }
  SB();
#pragma unroll
  for (int t = 0; t < 3; ++t)
#pragma unroll
    for (int s = 0; s < 4; ++s)
#pragma unroll
      for (int e = 0; e < 4; ++e) qf[t][s][e] = pack2(acc[t][2 * e][s], acc[t][2 * e + 1][s]);
}

__global__ __launch_bounds__(512, 2) void attn_kernel(AttnArgs a) {
  extern __shared__ __attribute__((aligned(16))) char smem[];
  const int lane = threadIdx.x & 63;
  const int w = __builtin_amdgcn_readfirstlane(threadIdx.x >> 6);
  const int xcd = blockIdx.x & 7, j = blockIdx.x >> 3;
  const bool type1 = j < 16;
  const int b = j & 15;
  const unsigned lds0 = (unsigned)(uintptr_t)(LAS char*)smem;
  const int nst = type1 ? 2 : 3, rdepth = type1 ? 5 : 3;
  const unsigned xoff = type1 ? XOFF1 : XOFF2;

  if (w >= (type1 ? 5 : 4)) {
    const unsigned g = lane >> 4, r = lane & 15;
    unsigned t0 = 0, t1 = 0, t2 = 0, t3 = 0;
    unsigned tq[8];
#pragma unroll
    for (int q = 0; q < 8; ++q) tq[q] = 0;
    if (WQ_PF3 == 2 && type1 && w == 5) {
      const unsigned vo = (unsigned)((32 * b + (lane >> 3)) * (HD * 2) + xcd * 1024 + (lane & 7) * 128);
      const unsigned v1 = vo + 8u * HD * 2, v2 = vo + 16u * HD * 2, v3 = vo + 24u * HD * 2;
      asm volatile(
          "s_nop 4\n\t"
          "global_load_dword %0, %8, %13\n\tglobal_load_dword %1, %9, %13\n\tglobal_load_dword %2, %10, %13\n\tglobal_load_dword %3, %11, %13\n\t"
          "global_load_dword %4, %8, %12\n\tglobal_load_dword %5, %9, %12\n\tglobal_load_dword %6, %10, %12\n\tglobal_load_dword %7, %11, %12"
          : "=&v"(tq[0]), "=&v"(tq[1]), "=&v"(tq[2]), "=&v"(tq[3]), "=&v"(tq[4]), "=&v"(tq[5]), "=&v"(tq[6]), "=&v"(tq[7])
          : "v"(vo), "v"(v1), "v"(v2), "v"(v3), "s"(a.wq[0]), "s"(a.wq[1])
          : "memory");
    }
    if (w == 7 && !NO_WQ_PF && !WQ_PF3) {
      const unsigned woff = (unsigned)((16 * j + (lane >> 3)) * (HD * 2) + xcd * 1024 + (lane & 7) * 128);
      asm volatile(
          "s_nop 4\n\t"
          "global_load_dword %0, %4, %6\n\tglobal_load_dword %1, %5, %6\n\t"
          "global_load_dword %2, %4, %7\n\tglobal_load_dword %3, %5, %7"
          : "=&v"(t0), "=&v"(t1), "=&v"(t2), "=&v"(t3)
          : "v"(woff), "v"(woff + 8u * HD * 2), "s"(a.wq[0]), "s"(a.wq[1])
          : "memory");
    }
    const int li = 7 - w;
    {
      const char* xg = a.x[type1 ? 0 : 1] + (size_t)b * (SQ * DM * 2);
      const int nq = type1 ? 4 : 3;
      for (int q = 0; q < nq; ++q) {
        const int rg = li * nq + q;
        const unsigned base = g * 256 + 16 * (r ^ (unsigned)(rg & 3));
        dma4c(xg + rg * 4096, lds0 + xoff + rg * 4096, base, base ^ 64u, base ^ 128u, base ^ 192u);
      }
    }
    const int nu = (type1 && li == 2) ? 2 : 3;
    const char* up[3];
    unsigned ul[3], uo[3][4];
    bool uA[3];
#pragma unroll
    for (int u = 0; u < 3; ++u) {
      const int id = (li * 3 + u) & (type1 ? 7 : 15);
      const int st = id >> 2, isv = (id >> 1) & 1, hf = id & 1;
      uA[u] = type1 && st == 1;
      const size_t sidx = uA[u] ? (size_t)(b * 8 + xcd) : (size_t)(b * 32 + 4 * xcd + (type1 ? 0 : 1 + st));
      const char* kb = uA[u] ? a.k[0] : a.k[1];
      const char* vb = uA[u] ? a.v[0] : a.v[1];
      up[u] = (isv ? vb : kb) + sidx * (SK * DH * 2) + hf * 4096;
      ul[u] = lds0 + st * BLK_BYTES + isv * 8192 + hf * 4096;
#pragma unroll
      for (int q = 0; q < 4; ++q) uo[u][q] = g * 256 + 16 * (r ^ (isv ? (((q & 1) * 4 + g) << 1) : ((g << 2) | q)));
    }
    const unsigned sstride = nst * BLK_BYTES;
    auto issue = [&](int blk, int slot, int which) {
#pragma unroll
      for (int u = 0; u < 3; ++u)
        if (u < nu && (which == 0 || (which == 2) == uA[u])) dma4(up[u] + blk * 8192, ul[u] + slot * sstride, uo[u][0], uo[u][1], uo[u][2], uo[u][3]);
    };
    int slot = rdepth - 1;
    if (type1) {
      issue(0, 0, 0); issue(1, 1, 0); issue(2, 2, 0); issue(3, 3, 1);
      u32x4 rb[NRB][3][4];
#pragma unroll
      for (int u = 0; u < 3; ++u) {
        const rsrc_t rs = mkrs(up[u < nu ? u : 0], SK * DH * 2 - 4096);
#pragma unroll
        for (int k = 0; k < NRB; ++k)
#pragma unroll
          for (int q = 0; q < 4; ++q)
            rb[k][u][q] = (u < nu) ? __builtin_amdgcn_raw_buffer_load_b128(rs, uo[u][q] + q * 1024, (RB0 + k * RBS) * 8192, 2) : u32x4{0, 0, 0, 0};
      }
      asm volatile("s_waitcnt vmcnt(%0)" ::"n"(8 * NRB) : "memory");
      BAR();
      BAR();
      WAIT_VM(0);
#pragma unroll
      for (int k = 0; k < NRB; ++k)
#pragma unroll
        for (int u = 0; u < 3; ++u) asm volatile("" : "+v"(rb[k][u][0]), "+v"(rb[k][u][1]), "+v"(rb[k][u][2]), "+v"(rb[k][u][3]));
      auto isreg = [&](int blk) { return blk >= RB0 && blk < RB0 + NRB * RBS && (blk - RB0) % RBS == 0; };
      for (int i = 0; i < NBLK; ++i) {
        int cnt = 0;
#pragma unroll
        for (int d = 1; d <= 3; ++d) cnt += (i + d < NBLK && !isreg(i + d)) ? 1 : 0;
        if (nu == 3) { if (cnt == 3) WAIT_VM(36); else if (cnt == 2) WAIT_VM(24); else if (cnt == 1) WAIT_VM(12); else WAIT_VM(0); }
        else { if (cnt == 3) WAIT_VM(24); else if (cnt == 2) WAIT_VM(16); else if (cnt == 1) WAIT_VM(8); else WAIT_VM(0); }
        BAR();
        if (i == 0) issue(3, 3, 2);
        if (isreg(i + 4)) {
#pragma unroll
          for (int k = 0; k < NRB; ++k)
            if (i + 4 == RB0 + k * RBS) {
#pragma unroll
              for (int u = 0; u < 3; ++u)
                if (u < nu)
#pragma unroll
                  for (int q = 0; q < 4; ++q)
                    *(LAS u32x4*)(uintptr_t)(ul[u] + slot * sstride + q * 1024 + lane * 16) = rb[k][u][q];
            }
          WAIT_LGKM0();
        } else if (i + 4 < NBLK) {
          issue(i + 4, slot, 0);
        }
        slot = (slot + 1 == rdepth) ? 0 : slot + 1;
      }
    } else {
      WAIT_VM(0);
      BAR();
      BAR();
      issue(0, 0, 0); issue(1, 1, 0);
      for (int i = 0; i < NBLK; ++i) {
        if (i + 1 < NBLK) WAIT_VM(12); else WAIT_VM(0);
        BAR();
        if (i + 2 < NBLK) issue(i + 2, slot, 0);
        slot = (slot + 1 == rdepth) ? 0 : slot + 1;
      }
    }
    asm volatile("" ::"v"(t0), "v"(t1), "v"(t2), "v"(t3));
#pragma unroll
    for (int q = 0; q < 8; ++q) asm volatile("" ::"v"(tq[q]));
    return;
  }
  if (!type1 && w == 3) {
    unsigned acc = 0;
    if (WQ_PF3 == 1) {
      const rsrc_t r0 = mkrs(a.wq[0], DM * HD * 2), r1 = mkrs(a.wq[1], DM * HD * 2);
      const unsigned vo = (unsigned)((32 * b + (lane >> 3)) * (HD * 2) + xcd * 1024 + (lane & 7) * 128);
      unsigned t[8];
#pragma unroll
      for (int q = 0; q < 4; ++q) {
        t[2 * q] = __builtin_amdgcn_raw_buffer_load_b32(r0, vo, q * 8 * (HD * 2), WQPF_AUX);
        t[2 * q + 1] = __builtin_amdgcn_raw_buffer_load_b32(r1, vo, q * 8 * (HD * 2), WQPF_AUX);
      }
      BAR();
#pragma unroll
      for (int q = 0; q < 8; ++q) asm volatile("" ::"v"(t[q]));
    } else {
      BAR();
    }
    BAR();
    for (int i = 0; i < WO_PF_IT; ++i) BAR();
    const rsrc_t rw = mkrs(a.wo[xcd & 1], HD * DM * 2);
    const unsigned vo = (unsigned)((256 * b + (lane >> 2)) * (DM * 2) + (xcd >> 2) * 512 + (lane & 3) * 128);
    unsigned t[16];
#pragma unroll
    for (int q = 0; q < 16; ++q) t[q] = __builtin_amdgcn_raw_buffer_load_b32(rw, vo, q * 16 * (DM * 2), 0);
#pragma unroll
    for (int q = 0; q < 16; ++q) asm volatile("" ::"v"(t[q]));
    (void)acc;
    return;
  }

  const bool roleA = type1 && w < 4;
  const int p = roleA ? 0 : 1;
  const int h = roleA ? 4 * xcd + w : (type1 ? 4 * xcd : 4 * xcd + 1 + w);
  const unsigned ringoff = (unsigned)((type1 ? (roleA ? 1 : 0) : w) * BLK_BYTES);
  const bool xlds = !(type1 && w == 4);

  u32x4 qf[3][4];
  if (xlds) qproj<true>(qf, a.wq[p] + h * 256, DM * HD * 2 - h * 256, a.x[p] + b * (SQ * DM * 2), smem, xoff, lane);
  else qproj<false>(qf, a.wq[p] + h * 256, DM * HD * 2 - h * 256, a.x[p] + b * (SQ * DM * 2), smem, xoff, lane);

  int lane2 = lane;
  asm volatile("" : "+v"(lane2));
  const int g = lane2 >> 4, r = lane2 & 15;
  f32x4 oacc[3][8];
#pragma unroll
  for (int t = 0; t < 3; ++t)
#pragma unroll
    for (int c = 0; c < 8; ++c) oacc[t][c] = f32x4{0.f, 0.f, 0.f, 0.f};
  float mrow[3] = {-__builtin_inff(), -__builtin_inff(), -__builtin_inff()};
  float thr[3] = {-__builtin_inff(), -__builtin_inff(), -__builtin_inff()};
  f32x4 lsum[3] = {f32x4{0.f, 0.f, 0.f, 0.f}, f32x4{0.f, 0.f, 0.f, 0.f}, f32x4{0.f, 0.f, 0.f, 0.f}};
  u32x4 ones = {0x3f803f80u, 0x3f803f80u, 0x3f803f80u, 0x3f803f80u};
  asm volatile("" : "+v"(ones));
  const unsigned fk = ((r & 3) << 2) | (r >> 2);
  unsigned kra[4];
#pragma unroll
  for (int s = 0; s < 4; ++s) kra[s] = r * 256 + 16 * ((4 * g + s) ^ fk);
  const unsigned vrow = 4 * g + (r >> 2);
  const unsigned vra = 8192 + vrow * 256 + 8 * (r & 1);
  const unsigned vsw = (vrow & 7) << 1, vp = (r >> 1) & 1;

  int slot = 0;
  for (int i = 0; i < NBLK; ++i) {
    const unsigned sb = ringoff + slot * nst * BLK_BYTES;
    WAIT_LGKM0();
    BAR();
    f32x4 sc[3][2];
    u32x4 kf[8], vf[8], pf[3];
    float lm = 0.f, pv[8];
#pragma unroll
    for (int s = 0; s < 8; ++s) kf[s] = *(const LAS u32x4*)((LAS char*)smem + sb + kra[s & 3] + (s >> 2) * 4096);
    auto qk = [&](int t, int s) {
      sc[t][s >> 2] = mfma16(kf[s], qf[t][s & 3], (s & 3) ? sc[t][s >> 2] : f32x4{0.f, 0.f, 0.f, 0.f});
    };
    auto ldv = [&](int c) {
      const unsigned ad = sb + vra + 16 * ((2 * c + vp) ^ vsw);
      s16x4 l0 = __builtin_amdgcn_ds_read_tr16_b64_v4i16((LAS s16x4*)((LAS char*)smem + ad));
      s16x4 l1 = __builtin_amdgcn_ds_read_tr16_b64_v4i16((LAS s16x4*)((LAS char*)smem + ad + 4096));
      u32x2 u0 = __builtin_bit_cast(u32x2, l0), u1 = __builtin_bit_cast(u32x2, l1);
      vf[c] = u32x4{u0[0], u0[1], u1[0], u1[1]};
    };
    auto mx = [&](int t, int q) {
      if (q == 0) lm = max3(sc[t][0][0], sc[t][0][1], sc[t][0][2]);
      else if (q == 1) lm = max3(lm, sc[t][0][3], sc[t][1][0]);
      else if (q == 2) lm = max3(lm, sc[t][1][1], sc[t][1][2]);
      else lm = __builtin_elementwise_maximum(lm, sc[t][1][3]);
    };
    auto rescale = [&](int t) {
      if (__builtin_expect(__any(!(lm <= thr[t])), 0)) {
        const float mn = fmaxf(mrow[t], xg_max(lm) * SCL);
        const float al = __builtin_amdgcn_exp2f(mrow[t] - mn);
        mrow[t] = mn;
        thr[t] = (mn + RESCALE_THR) * (1.0f / SCL);
        lsum[t] *= al;
#pragma unroll
        for (int c = 0; c < 8; ++c) oacc[t][c] *= al;
      }
    };
    auto ex = [&](int t, int e) {
      pv[e] = __builtin_amdgcn_exp2f(fmaf(sc[t][e >> 2][e & 3], SCL, -mrow[t]));
      if (e & 1) {
        unsigned u = pack2(pv[e - 1], pv[e]);
        asm volatile("" : "+v"(u));
        pf[t][e >> 1] = u;
      } else {
        asm volatile("" : "+v"(pv[e]));
      }
    };
    auto pvm = [&](int t, int c) {
      if (c < 8) oacc[t][c] = mfma16(vf[c], pf[t], oacc[t][c]);
      else lsum[t] = mfma16(ones, pf[t], lsum[t]);
    };
    SB();
#pragma unroll
    for (int s = 0; s < 8; ++s) qk(0, s);
    SB();
#pragma unroll
    for (int s = 0; s < 8; ++s) {
      qk(1, s);
      if (s >= 2 && s < 6) mx(0, s - 2);
      SB();
    }
    rescale(0);
#pragma unroll
    for (int s = 0; s < 8; ++s) {
      qk(2, s);
      ldv(s);
      if (s & 1) ex(0, s >> 1);
      SB();
    }
#pragma unroll
    for (int s = 0; s < 4; ++s) { ex(0, 4 + s); mx(1, s); SB(); }
    rescale(1);
#pragma unroll
    for (int s = 0; s < 9; ++s) {
      pvm(0, s);
      if (s < 8) ex(1, s);
      if (s >= 5) mx(2, s - 5);
      SB();
    }
    rescale(2);
#pragma unroll
    for (int s = 0; s < 9; ++s) {
      pvm(1, s);
      if (s < 8) ex(2, s);
      SB();
    }
#pragma unroll
    for (int s = 0; s < 9; ++s) pvm(2, s);
    slot = (slot + 1 == rdepth) ? 0 : slot + 1;
  }

  char* op = a.o + ((size_t)(p * 512 + b * 32 + h) * 12) * 1024 + lane2 * 16;
#pragma unroll
  for (int t = 0; t < 3; ++t) {
    const float inv = 1.0f / lsum[t][0];
#pragma unroll
    for (int m = 0; m < 4; ++m) {
      u32x4 f;
      f[0] = pack2(oacc[t][2 * m][0] * inv, oacc[t][2 * m][1] * inv);
      f[1] = pack2(oacc[t][2 * m][2] * inv, oacc[t][2 * m][3] * inv);
      f[2] = pack2(oacc[t][2 * m + 1][0] * inv, oacc[t][2 * m + 1][1] * inv);
      f[3] = pack2(oacc[t][2 * m + 1][2] * inv, oacc[t][2 * m + 1][3] * inv);
      *(u32x4*)(op + (t * 4 + m) * 1024) = f;
    }
  }
}

struct OutArgs {
  const char* wo[2];
  const char* o;
  char* out;
};
#define OP_PHASE 0
constexpr int OPD = 5;
constexpr int LDS_OPROJ = 8 * OPD * 4096;
__global__ __launch_bounds__(512, 2) void oproj_kernel(OutArgs a) {
  extern __shared__ __attribute__((aligned(16))) char smem[];
  const int lane = threadIdx.x & 63;
  const int w = __builtin_amdgcn_readfirstlane(threadIdx.x >> 6);
  const int xcd = blockIdx.x & 7, j = blockIdx.x >> 3;
  const int p = xcd & 1, b = ((xcd >> 1) & 1) * 8 + (j & 7), nb = (xcd >> 2) * 4 + (j >> 3);
  const int g = lane >> 4, r = lane & 15;
  const int ph = OP_PHASE ? (2 * (j & 7) + 4 * (j >> 3)) : 0;
  const char* wg = a.wo[p] + (size_t)w * 512 * (DM * 2) + nb * 128;
  const char* og = a.o + (size_t)(p * 512 + b * 32 + 4 * w) * 12288;
  const unsigned lw = (unsigned)(uintptr_t)(LAS char*)smem + w * (OPD * 4096);
  const unsigned so = (lane >> 3) * (DM * 2) + 16 * ((lane & 7) ^ (g << 1));
  const unsigned so1 = so + 7168, so2 = so + 2 * 7168, so3 = so + 3 * 7168, ov = lane * 16;
  const unsigned trow = 4 * g + (r >> 2);
  const unsigned tra = w * (OPD * 4096) + trow * 128 + 8 * (r & 1), tsw = ((trow >> 1) & 3) << 1, tp = (r >> 1) & 1;
  f32x4 acc[3][4];
#pragma unroll
  for (int t = 0; t < 3; ++t)
#pragma unroll
    for (int e = 0; e < 4; ++e) acc[t][e] = f32x4{0.f, 0.f, 0.f, 0.f};
  u32x4 ob[OPD][3];
  auto load = [&](int s) {
    const int q = (s + ph) & 15;
    dma4c(wg + q * (32 * DM * 2), lw + (s % OPD) * 4096, so, so1, so2, so3);
    const char* os = og + (q >> 2) * 12288 + (q & 3) * 1024 + 4096;
    asm volatile(
        "global_load_dwordx4 %0, %3, %4 offset:-4096\n\tglobal_load_dwordx4 %1, %3, %4\n\tglobal_load_dwordx4 %2, %3, %5"
        : "=&v"(ob[s % OPD][0]), "=&v"(ob[s % OPD][1]), "=&v"(ob[s % OPD][2])
        : "v"(ov), "s"(os), "s"(os + 4096)
        : "memory");
  };
#pragma unroll
  for (int s = 0; s < OPD; ++s) load(s);
#pragma unroll
  for (int s = 0; s < 16; ++s) {
    const int left = (15 - s < OPD - 1) ? 15 - s : OPD - 1;
    asm volatile("s_waitcnt vmcnt(%3)" : "+v"(ob[s % OPD][0]), "+v"(ob[s % OPD][1]), "+v"(ob[s % OPD][2]) : "n"(7 * left) : "memory");
    u32x4 f[4];
#pragma unroll
    for (int e = 0; e < 4; ++e) {
      const unsigned ad = tra + (s % OPD) * 4096 + 16 * ((2 * e + tp) ^ tsw);
      s16x4 l0 = __builtin_amdgcn_ds_read_tr16_b64_v4i16((LAS s16x4*)((LAS char*)smem + ad));
      s16x4 l1 = __builtin_amdgcn_ds_read_tr16_b64_v4i16((LAS s16x4*)((LAS char*)smem + ad + 2048));
      u32x2 u0 = __builtin_bit_cast(u32x2, l0), u1 = __builtin_bit_cast(u32x2, l1);
      f[e] = u32x4{u0[0], u0[1], u1[0], u1[1]};
    }
#pragma unroll
    for (int e = 0; e < 4; ++e)
#pragma unroll
      for (int t = 0; t < 3; ++t) acc[t][e] = mfma16(f[e], ob[s % OPD][t], acc[t][e]);
    if (s + OPD < 16) {
      asm volatile("s_waitcnt lgkmcnt(0)" : "+v"(acc[2][3]) : : "memory");
      load(s + OPD);
    }
  }
  __syncthreads();
  f32x4* red = (f32x4*)smem;
#pragma unroll
  for (int t = 0; t < 3; ++t)
#pragma unroll
    for (int e = 0; e < 4; ++e) red[(w * 12 + t * 4 + e) * 64 + (((lane & 15) << 2) | (lane >> 4))] = acc[t][e];
  __syncthreads();
  if (w < 6) {
    const int t = w >> 1, e = (w & 1) * 2 + (lane & 2 ? 1 : 0), q = lane >> 2, gh = lane & 1;
    f32x4 s0 = f32x4{0.f, 0.f, 0.f, 0.f}, s1 = s0;
#pragma unroll
    for (int ww = 0; ww < 8; ++ww) {
      s0 += red[(ww * 12 + t * 4 + e) * 64 + q * 4 + 2 * gh];
      s1 += red[(ww * 12 + t * 4 + e) * 64 + q * 4 + 2 * gh + 1];
    }
    u32x4 f = {pack2(s0[0], s0[1]), pack2(s0[2], s0[3]), pack2(s1[0], s1[1]), pack2(s1[2], s1[3])};
    *(u32x4*)(a.out + ((size_t)(b * SQ + 16 * t + q) * 1024 + p * 512 + nb * 64 + 16 * e + 8 * gh) * 2) = f;
  }
}

extern "C" void kernel_launch(void* const* d_in, const int* in_sizes, int n_in, void* d_out, int out_size, void* d_ws,
                              size_t ws_size, hipStream_t stream) {
  (void)in_sizes; (void)n_in; (void)out_size; (void)ws_size;
  AttnArgs a;
  a.x[0] = (const char*)d_in[0]; a.k[0] = (const char*)d_in[1]; a.v[0] = (const char*)d_in[2]; a.wq[0] = (const char*)d_in[3];
  a.x[1] = (const char*)d_in[5]; a.k[1] = (const char*)d_in[6]; a.v[1] = (const char*)d_in[7]; a.wq[1] = (const char*)d_in[8];
  a.wo[0] = (const char*)d_in[4]; a.wo[1] = (const char*)d_in[9];
  a.o = (char*)d_ws;
  OutArgs oa;
  oa.wo[0] = (const char*)d_in[4]; oa.wo[1] = (const char*)d_in[9]; oa.o = (const char*)d_ws; oa.out = (char*)d_out;
  (void)hipFuncSetAttribute((const void*)attn_kernel, hipFuncAttributeMaxDynamicSharedMemorySize, LDS_ATTN);
  (void)hipFuncSetAttribute((const void*)oproj_kernel, hipFuncAttributeMaxDynamicSharedMemorySize, LDS_OPROJ);
  hipLaunchKernelGGL(attn_kernel, dim3(256), dim3(512), LDS_ATTN, stream, a);
  hipLaunchKernelGGL(oproj_kernel, dim3(256), dim3(512), LDS_OPROJ, stream, oa);
}
```
